```python
import torch

def _softmax(x, axis=-1):
    return torch.softmax(x, dim=axis)
def _gelu(x):
    return torch.nn.functional.gelu(x, approximate="tanh")
def _silu(x):
    return torch.nn.functional.silu(x)
def _sigmoid(x):
    return torch.sigmoid(x)
def _layernorm(x, axis=-1, eps=1e-5):
    m = x.mean(dim=axis, keepdim=True)
    v = x.var(dim=axis, keepdim=True, unbiased=False)
    return (x - m) / torch.sqrt(v + eps)
def _scatter_add(tgt, idx, src, axis=0):
    return tgt.clone().index_add_(axis, idx, src)
def _segment_sum(src, seg, n):
    out = torch.zeros((n,) + src.shape[1:], dtype=src.dtype)
    return out.index_add_(0, seg, src)
def _pad(x, pw):
    flat = tuple(v for pair in reversed(pw) for v in pair)
    return torch.nn.functional.pad(x, flat)

kernel_name = 'recurrent_scan__gqa_block_cached__mixer_channel_mlp__prompt'

def setup_inputs():

    _w = (lambda a: (lambda t: t.to(torch.bfloat16) if t.is_floating_point() else t)(torch.as_tensor(a)))
    def _A_setup():
        B, Sq, D = 1, 2048, 640
        Hq, Hkv, Sk, Dh = 32, 32, 2048, 192
        assert Hq % Hkv == 0, (Hq, Hkv)
        return {
            "x": _w((lambda *s: torch.randn(*s, dtype=torch.bfloat16) * 0.05)(B, Sq, D)),
            "k_cache": _w((lambda *s: torch.randn(*s, dtype=torch.bfloat16) * 1.6)(B, Hkv, Sk, Dh)),
            "v_cache": _w((lambda *s: torch.randn(*s, dtype=torch.bfloat16) * 0.8)(B, Hkv, Sk, Dh)),
            "wq": _w((lambda *s: torch.randn(*s, dtype=torch.bfloat16) * 1.6)(D, Hq * Dh) * (D ** -0.5)),
            "wo": _w((lambda *s: torch.randn(*s, dtype=torch.bfloat16) * 0.8)(Hq * Dh, D) * ((Hq * Dh) ** -0.5)),
        }
    _A_in = _A_setup()

    def _B_setup():
        B, S, D, F = 1, 2048, 640, 1536
        return {
            "x": _w((lambda *s: torch.randn(*s, dtype=torch.bfloat16) * 0.05)(B, S, D)),
            "w1": _w((lambda *s: torch.randn(*s, dtype=torch.bfloat16) * 3.2)(D, F) * (D ** -0.5)),
            "b1": _w((lambda *s: torch.randn(*s, dtype=torch.bfloat16) * 1.6)(F)),
            "w2": _w((lambda *s: torch.randn(*s, dtype=torch.bfloat16) * 0.2)(F, D) * (F ** -0.5)),
            "b2": _w((lambda *s: torch.randn(*s, dtype=torch.bfloat16) * 0.2)(D)),
        }
    _B_in = _B_setup()

    return {
        "A_x": _A_in["x"],
        "A_k_cache": _A_in["k_cache"],
        "A_v_cache": _A_in["v_cache"],
        "A_wq": _A_in["wq"],
        "A_wo": _A_in["wo"],
        "B_x": _B_in["x"],
        "B_w1": _B_in["w1"],
        "B_b1": _B_in["b1"],
        "B_w2": _B_in["w2"],
        "B_b2": _B_in["b2"],
    }

def reference(A_x, A_k_cache, A_v_cache, A_wq, A_wo, B_x, B_w1, B_b1, B_w2, B_b2):
    def _A_ref(x, k_cache, v_cache, wq, wo):
        B, Sq, D = x.shape
        Hq, Dh = 32, 192
        Hkv = k_cache.shape[1]
        rep = Hq // Hkv
        q = (lambda x, *d: x.permute(*d))((x @ wq).reshape(B, Sq, Hq, Dh), 0, 2, 1, 3)
        k = (lambda x, n, axis: torch.repeat_interleave(x, n, dim=axis))(k_cache, rep, axis=1)
        v = (lambda x, n, axis: torch.repeat_interleave(x, n, dim=axis))(v_cache, rep, axis=1)
        scores = torch.einsum("bhqd,bhkd->bhqk", q, k) * (Dh ** -0.5)
        p = _softmax(scores, axis=-1)
        o = torch.einsum("bhqk,bhkd->bhqd", p, v)
        return (lambda x, *d: x.permute(*d))(o, 0, 2, 1, 3).reshape(B, Sq, Hq * Dh) @ wo

    def _B_ref(x, w1, b1, w2, b2):
        y = _layernorm(x, axis=-1)
        h = _gelu(torch.einsum("bsd,df->bsf", y, w1) + b1)
        return torch.einsum("bsf,fd->bsd", h, w2) + b2

    x = A_x
    xn = _layernorm(x, axis=-1)
    s = _A_ref(xn, A_k_cache, A_v_cache, A_wq, A_wo)
    g = _silu(_B_ref(xn, B_w1, B_b1, B_w2, B_b2))
    return x + s * g
```

```cpp
#include <hip/hip_runtime.h>
#include <hip/hip_bf16.h>
#include <stdint.h>

using bf16x8 = __attribute__((ext_vector_type(8))) short;
using s16x4  = __attribute__((ext_vector_type(4))) short;
using f32x16 = __attribute__((ext_vector_type(16))) float;
using f32x4  = __attribute__((ext_vector_type(4))) float;
using u32x4  = __attribute__((ext_vector_type(4))) unsigned;
using u32x2  = __attribute__((ext_vector_type(2))) unsigned;
typedef short v4i16_t __attribute__((ext_vector_type(4)));
typedef __attribute__((address_space(3))) const char* lds_cptr;
typedef unsigned short u16;

constexpr int SEQ = 2048, DM = 640, NH = 32, DH = 192, DQ = 6144, FF = 1536;

#define DI __device__ __forceinline__
#define SBAR() __builtin_amdgcn_sched_barrier(0)
#define PIN(x) asm volatile("" : "+v"(x))
#define MFMA(a, b, c) __builtin_amdgcn_mfma_f32_32x32x16_bf16(a, b, c, 0, 0, 0)
#define WAIT_BAR(N) asm volatile("s_waitcnt vmcnt(" #N ") lgkmcnt(0)\n\ts_barrier" ::: "memory")

DI float bf2f(u16 b) { return __uint_as_float((unsigned)b << 16); }
typedef float f32x2 __attribute__((ext_vector_type(2)));
typedef __bf16 bf16x2_t __attribute__((ext_vector_type(2)));
DI unsigned cvtpk(float lo, float hi) { f32x2 v = {lo, hi}; bf16x2_t b = __builtin_convertvector(v, bf16x2_t); return __builtin_bit_cast(unsigned, b); }
DI float rbf(float x) { return __uint_as_float(cvtpk(x, x) << 16); }
DI void glds16(const void* g, unsigned lds_base) {
    unsigned sv;
    asm volatile("s_mov_b32 %0, m0\n\ts_mov_b32 m0, %2\n\ts_nop 0\n\tglobal_load_lds_dwordx4 %1, off\n\ts_mov_b32 m0, %0" : "=&s"(sv) : "v"(g), "s"(lds_base) : "memory");
}
DI bf16x8 ldsr(lds_cptr p) { return *(const __attribute__((address_space(3))) bf16x8*)p; }
DI s16x4 vtr(lds_cptr p) { return __builtin_bit_cast(s16x4, __builtin_amdgcn_ds_read_tr16_b64_v4i16((__attribute__((address_space(3))) v4i16_t*)p)); }
DI bf16x8 cat(s16x4 a, s16x4 b) { return __builtin_shufflevector(a, b, 0, 1, 2, 3, 4, 5, 6, 7); }
DI unsigned rfl(unsigned x) { return (unsigned)__builtin_amdgcn_readfirstlane((int)x); }
DI float xhalf_max(float m) { auto rr = __builtin_amdgcn_permlane32_swap(__float_as_uint(m), __float_as_uint(m), false, false); return __builtin_fmaxf(__uint_as_float(rr[0]), __uint_as_float(rr[1])); }
DI float xhalf_sum(float m) { auto rr = __builtin_amdgcn_permlane32_swap(__float_as_uint(m), __float_as_uint(m), false, false); return __uint_as_float(rr[0]) + __uint_as_float(rr[1]); }

DI float wave_sum(float v) {
#pragma unroll
    for (int o = 32; o > 0; o >>= 1) v += __shfl_xor(v, o);
    return v;
}
__global__ __launch_bounds__(256) void k0_ln(const u16* __restrict__ x, u16* __restrict__ xn, u16* __restrict__ y) {
    const int lane = threadIdx.x & 63, row = blockIdx.x * 4 + (threadIdx.x >> 6);
    const unsigned* xr = (const unsigned*)(x + (size_t)row * DM);
    float v[10];
#pragma unroll
    for (int i = 0; i < 5; ++i) { unsigned w = xr[lane + 64 * i]; v[2 * i] = __uint_as_float(w << 16); v[2 * i + 1] = __uint_as_float(w & 0xffff0000u); }
#pragma unroll
    for (int pass = 0; pass < 2; ++pass) {
        float s = 0.f;
#pragma unroll
        for (int i = 0; i < 10; ++i) s += v[i];
        const float mean = wave_sum(s) * (1.0f / DM);
        float q = 0.f;
#pragma unroll
        for (int i = 0; i < 10; ++i) { float d = v[i] - mean; q += d * d; }
        const float var = wave_sum(q) * (1.0f / DM);
        const float mb = rbf(mean), vb = rbf(var);
        const float den = rbf(__fsqrt_rn(rbf(vb + 1e-5f)));
#pragma unroll
        for (int i = 0; i < 10; ++i) v[i] = rbf(__fdiv_rn(rbf(v[i] - mb), den));
        unsigned* dst = (unsigned*)((pass == 0 ? xn : y) + (size_t)row * DM);
#pragma unroll
        for (int i = 0; i < 5; ++i) dst[lane + 64 * i] = (__float_as_uint(v[2 * i]) >> 16) | (__float_as_uint(v[2 * i + 1]) & 0xffff0000u);
    }
}

constexpr int SLOT = 24576, LDS_K = 0, LDS_V = 3 * SLOT, K1_LDS = 6 * SLOT;
constexpr float CSC = 0.07216878364870322f * 1.4426950408889634f;
constexpr float THR = 8.f;

DI float gelu_tanh(float x) {
    const float u = 0.7978845608028654f * (x + 0.044715f * x * x * x);
    const float e = __builtin_amdgcn_exp2f(u * 2.8853900817779268f);
    const float t = 1.f - 2.f * __builtin_amdgcn_rcpf(e + 1.f);
    return 0.5f * x * (1.f + t);
}

__global__ __launch_bounds__(512, 2)
void k1_attn(const u16* __restrict__ xn, const u16* __restrict__ yln, const u16* __restrict__ Kc, const u16* __restrict__ Vc,
             const u16* __restrict__ wq, const u16* __restrict__ w1, const u16* __restrict__ b1,
             u16* __restrict__ o, u16* __restrict__ hbuf) {
    extern __shared__ __attribute__((aligned(16))) char lds[];
    const int tid = threadIdx.x, lane = tid & 63, r32 = lane & 31, hi = lane >> 5;
    const int wid = __builtin_amdgcn_readfirstlane(tid >> 6);
    const int bid = blockIdx.x, xcd = bid & 7, jj = bid >> 3;
    const int head = xcd + 8 * (jj >> 3), qb = jj & 7;
    const int row0 = qb * 256 + wid * 32;
    const unsigned lds0 = (unsigned)(uintptr_t)lds;
    const lds_cptr L = (lds_cptr)lds;

    const int vd1 = lane >> 5, vr7 = (lane >> 2) & 7, vch = lane & 3;
    const int kr7 = ((lane >> 4) << 1) | (((lane >> 3) & 1) ^ ((wid >> 1) & 1)), kc8 = (lane & 7) ^ kr7;
    const int q4 = (lane & 15) >> 2, p4 = lane & 3, g1 = (lane >> 4) & 1;
    const int r7 = r32 & 7;

    f32x16 o6[6];
    f32x16 hacc[2];
#pragma unroll
    for (int i = 0; i < 6; ++i) o6[i] = f32x16{};
    hacc[0] = f32x16{}; hacc[1] = f32x16{};
    const int fcol0 = (48 * head < FF - 64) ? 48 * head : FF - 64;
    {
        const char* wqs = (const char*)(wq + (size_t)(8 * wid + vr7) * DQ + head * DH) + vd1 * 64 + vch * 16;
        const char* w1s = (const char*)(w1 + (size_t)(8 * wid + vr7) * FF + fcol0) + vd1 * 64 + vch * 16;
        const unsigned wqd = lds0 + LDS_V + wid * 3072, w1d = lds0 + LDS_K + wid * 1024;
#define DMA_W(c, slot) do { \
            glds16(wqs + (size_t)(c) * 64 * DQ * 2,       rfl(wqd + (slot))); \
            glds16(wqs + (size_t)(c) * 64 * DQ * 2 + 128, rfl(wqd + (slot) + 1024)); \
            glds16(wqs + (size_t)(c) * 64 * DQ * 2 + 256, rfl(wqd + (slot) + 2048)); \
            glds16(w1s + (size_t)(c) * 64 * FF * 2,       rfl(w1d + (slot))); } while (0)
        const int p4s = ((p4 & 1) << 1) | (p4 >> 1);
        const lds_cptr wqp = L + LDS_V + hi * 3072 + q4 * 64 + g1 * 32 + p4s * 8;
        const lds_cptr w1p = L + LDS_K + hi * 1024 + q4 * 64 + g1 * 32 + p4 * 8;
        const u16* xrow = xn + (size_t)(row0 + r32) * DM + hi * 8;
        const u16* yrow = yln + (size_t)(row0 + r32) * DM + hi * 8;
        DMA_W(0, 0); DMA_W(1, SLOT);
        bf16x8 xf[4], yf[4], xg[4], yg[4];
#pragma unroll
        for (int s = 0; s < 4; ++s) { xf[s] = *(const bf16x8*)(xrow + 16 * s); yf[s] = *(const bf16x8*)(yrow + 16 * s); }
        WAIT_BAR(4);
        int sl = 0;
        for (int c = 0; c < 10; ++c) {
            const int sl2 = (sl >= SLOT) ? sl - SLOT : sl + 2 * SLOT;
            if (c + 2 < 10) DMA_W(c + 2, sl2);
            const int cn = (c + 1 < 10) ? c + 1 : c;
#pragma unroll
            for (int s = 0; s < 4; ++s) { xg[s] = *(const bf16x8*)(xrow + 64 * cn + 16 * s); yg[s] = *(const bf16x8*)(yrow + 64 * cn + 16 * s); }
#pragma unroll
            for (int s = 0; s < 4; ++s) {
#pragma unroll
                for (int db = 0; db < 6; ++db) {
                    const int off = sl + 2 * s * 3072 + (db >> 1) * 1024 + (db & 1) * 512;
                    bf16x8 wf = cat(vtr(wqp + off), vtr(wqp + off + 256));
                    o6[db] = MFMA(wf, xf[s], o6[db]);
                }
#pragma unroll
                for (int ft = 0; ft < 2; ++ft) {
                    const int off = sl + 2 * s * 1024 + ft * 512;
                    bf16x8 wf = cat(vtr(w1p + off), vtr(w1p + off + 256));
                    hacc[ft] = MFMA(wf, yf[s], hacc[ft]);
                }
            }
#pragma unroll
            for (int s = 0; s < 4; ++s) { xf[s] = xg[s]; yf[s] = yg[s]; }
            WAIT_BAR(0);
            sl = (sl == 2 * SLOT) ? 0 : sl + SLOT;
        }
#undef DMA_W
    }
    const char* ksrc = (const char*)(Kc + (size_t)head * SEQ * DH) + kc8 * 16;
    const char* vsrc = (const char*)(Vc + (size_t)head * SEQ * DH) + vd1 * 64 + vch * 16;
    const unsigned kdst = lds0 + LDS_K + wid * 3072, vdst = lds0 + LDS_V + wid * 3072;
#define DMA_K(t, slot) do { const char* s_ = ksrc + (size_t)((64 * (t) + 32 + 8 * wid + kr7) & (SEQ - 1)) * (DH * 2); \
        glds16(s_, rfl(kdst + (slot))); glds16(s_ + 128, rfl(kdst + (slot) + 1024)); glds16(s_ + 256, rfl(kdst + (slot) + 2048)); } while (0)
#define DMA_V(t, slot) do { const char* s_ = vsrc + (size_t)((64 * (t) + 8 * wid + vr7) & (SEQ - 1)) * (DH * 2); \
        glds16(s_, rfl(vdst + (slot))); glds16(s_ + 128, rfl(vdst + (slot) + 1024)); glds16(s_ + 256, rfl(vdst + (slot) + 2048)); } while (0)
    DMA_K(-1, 2 * SLOT); DMA_K(0, 0); DMA_V(0, 0); DMA_K(1, SLOT); DMA_V(1, SLOT);

    bf16x8 qf[12];
#pragma unroll
    for (int db = 0; db < 6; ++db)
#pragma unroll
        for (int s = 0; s < 2; ++s) {
            u32x4 w;
#pragma unroll
            for (int e = 0; e < 4; ++e) w[e] = cvtpk(o6[db][8 * s + 2 * e], o6[db][8 * s + 2 * e + 1]);
            qf[2 * db + s] = __builtin_bit_cast(bf16x8, w);
        }
    {
        const int flo = 48 * head - fcol0;
        u16* hrow = hbuf + (size_t)(row0 + r32) * FF + fcol0;
#pragma unroll
        for (int ft = 0; ft < 2; ++ft)
#pragma unroll
            for (int g = 0; g < 4; ++g) {
                const int fl = 32 * ft + 8 * g + 4 * hi;
                if (fl >= flo && fl < flo + 48) {
                    const u32x2 bb = *(const u32x2*)(b1 + fcol0 + fl);
                    float hv[4];
#pragma unroll
                    for (int e = 0; e < 4; ++e) {
                        const float bias = (e & 1) ? __uint_as_float(bb[e >> 1] & 0xffff0000u) : __uint_as_float(bb[e >> 1] << 16);
                        hv[e] = gelu_tanh(rbf(rbf(hacc[ft][4 * g + e]) + bias));
                    }
                    u32x2 w; w[0] = cvtpk(hv[0], hv[1]); w[1] = cvtpk(hv[2], hv[3]);
                    *(u32x2*)(hrow + fl) = w;
                }
            }
    }
#pragma unroll
    for (int i = 0; i < 6; ++i) o6[i] = f32x16{};

    lds_cptr kp[4];
#pragma unroll
    for (int j = 0; j < 4; ++j)
        kp[j] = L + LDS_K + (r32 >> 3) * 3072 + ((r7 >> 1) << 8) + ((((r32 >> 4) & 1) ^ (r7 & 1)) << 7) + ((r7 ^ (2 * j + hi)) << 4);
    const lds_cptr vp = L + LDS_V + (4 * hi + q4) * 64 + g1 * 32 + p4 * 8;

    f32x16 SA, SB; float mref, lsum = 0.f;
    u32x4 P0, P1;
#define KRD(sl_, half, ks) ldsr(kp[(ks) & 3] + (sl_) + (half) * 12288 + ((ks) >> 2) * 1024)
#define VRD(sl_, half, s, db) cat(vtr(vp + (sl_) + (4 * (half) + 2 * (s)) * 3072 + ((db) >> 1) * 1024 + ((db) & 1) * 512), \
                                  vtr(vp + (sl_) + (4 * (half) + 2 * (s) + 1) * 3072 + ((db) >> 1) * 1024 + ((db) & 1) * 512))
    asm volatile("s_waitcnt vmcnt(12)\n\ts_barrier" ::: "memory");
    {
        SA = f32x16{};
#pragma unroll
        for (int ks = 0; ks < 12; ++ks) SA = MFMA(KRD(2 * SLOT, 1, ks), qf[ks], SA);
        float mx = SA[0];
#pragma unroll
        for (int i = 1; i < 16; ++i) mx = __builtin_fmaxf(mx, SA[i]);
        mref = xhalf_max(mx) * CSC;
    }
    WAIT_BAR(3);

#define EX(v) __builtin_amdgcn_exp2f(__builtin_fmaf((v), CSC, nm))
#define HSTEP(SC, SN, ksl, khalf, vsl, vhalf) do { \
        const float nm = -mref; \
        bf16x8 k0 = KRD(ksl, khalf, 0), k1 = KRD(ksl, khalf, 1), k2; SBAR(); \
        k2 = KRD(ksl, khalf, 2);  SN = MFMA(k0, qf[0], f32x16{}); SC[0] = EX(SC[0]); SC[1] = EX(SC[1]); PIN(SC); SBAR(); \
        k0 = KRD(ksl, khalf, 3);  SN = MFMA(k1, qf[1], SN); SC[2] = EX(SC[2]); SC[3] = EX(SC[3]); PIN(SC); SBAR(); \
        k1 = KRD(ksl, khalf, 4);  SN = MFMA(k2, qf[2], SN); SC[4] = EX(SC[4]); SC[5] = EX(SC[5]); PIN(SC); SBAR(); \
        k2 = KRD(ksl, khalf, 5);  SN = MFMA(k0, qf[3], SN); SC[6] = EX(SC[6]); SC[7] = EX(SC[7]); PIN(SC); SBAR(); \
        k0 = KRD(ksl, khalf, 6);  SN = MFMA(k1, qf[4], SN); SC[8] = EX(SC[8]); SC[9] = EX(SC[9]); P0[0] = cvtpk(SC[0], SC[1]); P0[1] = cvtpk(SC[2], SC[3]); PIN(SC); PIN(P0); SBAR(); \
        k1 = KRD(ksl, khalf, 7);  SN = MFMA(k2, qf[5], SN); SC[10] = EX(SC[10]); SC[11] = EX(SC[11]); P0[2] = cvtpk(SC[4], SC[5]); P0[3] = cvtpk(SC[6], SC[7]); PIN(SC); PIN(P0); SBAR(); \
        k2 = KRD(ksl, khalf, 8);  SN = MFMA(k0, qf[6], SN); SC[12] = EX(SC[12]); SC[13] = EX(SC[13]); PIN(SC); SBAR(); \
        k0 = KRD(ksl, khalf, 9);  SN = MFMA(k1, qf[7], SN); SC[14] = EX(SC[14]); SC[15] = EX(SC[15]); PIN(SC); SBAR(); \
        k1 = KRD(ksl, khalf, 10); SN = MFMA(k2, qf[8], SN); P1[0] = cvtpk(SC[8], SC[9]); P1[1] = cvtpk(SC[10], SC[11]); PIN(P1); SBAR(); \
        k2 = KRD(ksl, khalf, 11); SN = MFMA(k0, qf[9], SN); P1[2] = cvtpk(SC[12], SC[13]); P1[3] = cvtpk(SC[14], SC[15]); PIN(P1); SBAR(); \
        bf16x8 v0 = VRD(vsl, vhalf, 0, 0); SN = MFMA(k1, qf[10], SN); SBAR(); \
        bf16x8 v1 = VRD(vsl, vhalf, 0, 1); SN = MFMA(k2, qf[11], SN); SBAR(); \
        const bf16x8 pa0 = __builtin_bit_cast(bf16x8, P0), pa1 = __builtin_bit_cast(bf16x8, P1); \
        bf16x8 v2; float sa = SC[0] + SC[1], sb = SC[2] + SC[3], mx; \
        v2 = VRD(vsl, vhalf, 0, 2); o6[0] = MFMA(v0, pa0, o6[0]); sa += SC[4]; sb += SC[5]; PIN(sa); PIN(sb); SBAR(); \
        v0 = VRD(vsl, vhalf, 0, 3); o6[1] = MFMA(v1, pa0, o6[1]); sa += SC[6]; sb += SC[7]; PIN(sa); PIN(sb); SBAR(); \
        v1 = VRD(vsl, vhalf, 0, 4); o6[2] = MFMA(v2, pa0, o6[2]); sa += SC[8]; sb += SC[9]; mx = __builtin_fmaxf(__builtin_fmaxf(SN[0], SN[1]), SN[2]); PIN(sa); PIN(sb); PIN(mx); SBAR(); \
        v2 = VRD(vsl, vhalf, 0, 5); o6[3] = MFMA(v0, pa0, o6[3]); sa += SC[10]; sb += SC[11]; mx = __builtin_fmaxf(__builtin_fmaxf(mx, SN[3]), SN[4]); PIN(sa); PIN(sb); PIN(mx); SBAR(); \
        v0 = VRD(vsl, vhalf, 1, 0); o6[4] = MFMA(v1, pa0, o6[4]); sa += SC[12]; sb += SC[13]; mx = __builtin_fmaxf(__builtin_fmaxf(mx, SN[5]), SN[6]); PIN(sa); PIN(sb); PIN(mx); SBAR(); \
        v1 = VRD(vsl, vhalf, 1, 1); o6[5] = MFMA(v2, pa0, o6[5]); sa += SC[14]; sb += SC[15]; mx = __builtin_fmaxf(__builtin_fmaxf(mx, SN[7]), SN[8]); PIN(sa); PIN(sb); PIN(mx); SBAR(); \
        v2 = VRD(vsl, vhalf, 1, 2); o6[0] = MFMA(v0, pa1, o6[0]); mx = __builtin_fmaxf(__builtin_fmaxf(mx, SN[9]), SN[10]); lsum += sa + sb; PIN(mx); PIN(lsum); SBAR(); \
        v0 = VRD(vsl, vhalf, 1, 3); o6[1] = MFMA(v1, pa1, o6[1]); mx = __builtin_fmaxf(__builtin_fmaxf(mx, SN[11]), SN[12]); PIN(mx); SBAR(); \
        v1 = VRD(vsl, vhalf, 1, 4); o6[2] = MFMA(v2, pa1, o6[2]); mx = __builtin_fmaxf(__builtin_fmaxf(mx, SN[13]), SN[14]); PIN(mx); SBAR(); \
        v2 = VRD(vsl, vhalf, 1, 5); o6[3] = MFMA(v0, pa1, o6[3]); mx = __builtin_fmaxf(mx, SN[15]); PIN(mx); SBAR(); \
        o6[4] = MFMA(v1, pa1, o6[4]); mx = __builtin_fmaf(xhalf_max(mx), CSC, nm); PIN(mx); SBAR(); \
        o6[5] = MFMA(v2, pa1, o6[5]); SBAR(); \
        if (__builtin_expect(__any(mx > THR), 0)) { \
            const float dl = __builtin_fmaxf(mx, 0.f); mref += dl; const float f = __builtin_amdgcn_exp2f(-dl); lsum *= f; \
            _Pragma("unroll") for (int d_ = 0; d_ < 6; ++d_) _Pragma("unroll") for (int r_ = 0; r_ < 16; ++r_) o6[d_][r_] *= f; \
        } \
    } while (0)

    int sl = 0;
    for (int t = 0; t < 32; ++t) {
        const int sl2 = (sl >= SLOT) ? sl - SLOT : sl + 2 * SLOT;
        DMA_K(t + 2, sl2); DMA_V(t + 2, sl2);
        HSTEP(SA, SB, sl, 0, sl, 0);
        HSTEP(SB, SA, sl, 1, sl, 1);
        WAIT_BAR(3);
        sl = (sl == 2 * SLOT) ? 0 : sl + SLOT;
    }
    asm volatile("s_waitcnt vmcnt(0)" ::: "memory");

    {
        const float il = __builtin_amdgcn_rcpf(xhalf_sum(lsum));
        u16* orow = o + (size_t)(row0 + r32) * DQ + head * DH + 4 * hi;
#pragma unroll
        for (int db = 0; db < 6; ++db)
#pragma unroll
            for (int g = 0; g < 4; ++g) {
                u32x2 w; w[0] = cvtpk(o6[db][4 * g] * il, o6[db][4 * g + 1] * il); w[1] = cvtpk(o6[db][4 * g + 2] * il, o6[db][4 * g + 3] * il);
                *(u32x2*)(orow + 32 * db + 8 * g) = w;
            }
    }
}

constexpr int K2_STAGE = 73728, K2_A = 0, K2_W = 32768, K2_LDS = 2 * K2_STAGE;
__global__ __launch_bounds__(512, 2)
void k2_gemm(const u16* __restrict__ o, const u16* __restrict__ hbuf, const u16* __restrict__ wo, const u16* __restrict__ w2, float* __restrict__ part) {
    extern __shared__ __attribute__((aligned(16))) char lds[];
    const int tid = threadIdx.x, lane = tid & 63, r32 = lane & 31, hi = lane >> 5;
    const int wid = __builtin_amdgcn_readfirstlane(tid >> 6), wm = wid & 3, kh = wid >> 2;
    const int bid = blockIdx.x;
    const int chunk = bid & 3, tile = bid >> 2, tn = tile & 3, tm = tile >> 2;
    const u16* A; const u16* W; int lda, nst;
    if (chunk < 3) { A = o + (size_t)tm * 128 * DQ + chunk * 2048; lda = DQ; W = wo + (size_t)chunk * 2048 * DM + tn * 160; nst = 16; }
    else           { A = hbuf + (size_t)tm * 128 * FF; lda = FF; W = w2 + tn * 160; nst = 12; }
    const unsigned lds0 = (unsigned)(uintptr_t)lds;
    const lds_cptr L = (lds_cptr)lds;
    const char* asrc[2];
#pragma unroll
    for (int i = 0; i < 2; ++i) {
        const int a = 2 * wid + i;
        const int kr7 = ((lane >> 4) << 1) | (((lane >> 3) & 1) ^ ((a >> 1) & 1)), kc8 = (lane & 7) ^ kr7;
        asrc[i] = (const char*)(A + (size_t)(8 * a + kr7) * lda) + kc8 * 16;
    }
    const char* wsrc = (const char*)(W + (size_t)(16 * wid + (lane >> 2)) * DM) + (lane & 3) * 16;
    const unsigned adst = lds0 + K2_A + wid * 4096, wdst = lds0 + K2_W + wid * 5120;
#define DMA2(st, buf) do { \
        glds16(asrc[0] + (size_t)(st) * 256,       rfl(adst + (buf))); \
        glds16(asrc[0] + (size_t)(st) * 256 + 128, rfl(adst + (buf) + 1024)); \
        glds16(asrc[1] + (size_t)(st) * 256,       rfl(adst + (buf) + 2048)); \
        glds16(asrc[1] + (size_t)(st) * 256 + 128, rfl(adst + (buf) + 3072)); \
        _Pragma("unroll") for (int cb = 0; cb < 5; ++cb) glds16(wsrc + (size_t)(st) * 128 * DM * 2 + cb * 64, rfl(wdst + (buf) + cb * 1024)); } while (0)
    const int q4 = (lane & 15) >> 2, p4 = lane & 3, g1 = (lane >> 4) & 1, r7 = r32 & 7;
    lds_cptr ap[4];
#pragma unroll
    for (int j = 0; j < 4; ++j)
        ap[j] = L + K2_A + (4 * wm + (r32 >> 3)) * 2048 + kh * 1024 + ((r7 >> 1) << 8) + ((((r32 >> 4) & 1) ^ (r7 & 1)) << 7) + ((r7 ^ (2 * j + hi)) << 4);
    const lds_cptr wp = L + K2_W + kh * 4 * 5120 + (8 * hi + q4) * 64 + g1 * 32 + p4 * 8;
    f32x16 acc[5];
#pragma unroll
    for (int i = 0; i < 5; ++i) acc[i] = f32x16{};
    DMA2(0, 0);
    WAIT_BAR(0);
    for (int st = 0; st < nst; ++st) {
        const int buf = (st & 1) * K2_STAGE;
        if (st + 1 < nst) DMA2(st + 1, K2_STAGE - buf);
#pragma unroll
        for (int s = 0; s < 4; ++s) {
            const bf16x8 af = ldsr(ap[s] + buf);
#pragma unroll
            for (int nt = 0; nt < 5; ++nt) {
                const bf16x8 wf = cat(vtr(wp + buf + s * 5120 + nt * 1024), vtr(wp + buf + s * 5120 + nt * 1024 + 256));
                acc[nt] = MFMA(wf, af, acc[nt]);
            }
        }
        WAIT_BAR(0);
    }
    float* red = (float*)lds + (size_t)wm * 5 * 16 * 64;
    if (kh == 1) {
#pragma unroll
        for (int nt = 0; nt < 5; ++nt)
#pragma unroll
            for (int g = 0; g < 4; ++g) { f32x4 v = {acc[nt][4 * g], acc[nt][4 * g + 1], acc[nt][4 * g + 2], acc[nt][4 * g + 3]}; *(f32x4*)(red + ((nt * 4 + g) * 64 + lane) * 4) = v; }
    }
    __syncthreads();
    if (kh == 0) {
        float* prow = part + ((size_t)chunk * SEQ + tm * 128 + wm * 32 + r32) * DM + tn * 160 + 4 * hi;
#pragma unroll
        for (int nt = 0; nt < 5; ++nt)
#pragma unroll
            for (int g = 0; g < 4; ++g) {
                f32x4 v = *(const f32x4*)(red + ((nt * 4 + g) * 64 + lane) * 4);
                v[0] += acc[nt][4 * g]; v[1] += acc[nt][4 * g + 1]; v[2] += acc[nt][4 * g + 2]; v[3] += acc[nt][4 * g + 3];
                *(f32x4*)(prow + 32 * nt + 8 * g) = v;
            }
    }
}

__global__ __launch_bounds__(256) void k3_final(const float* __restrict__ part, const u16* __restrict__ x, const u16* __restrict__ b2, u16* __restrict__ out) {
    const int i = blockIdx.x * 256 + threadIdx.x;
    const size_t e = (size_t)i * 4; const int col = (int)(e % DM);
    constexpr size_t PS = (size_t)SEQ * DM;
    const f32x4 p0 = *(const f32x4*)(part + e), p1 = *(const f32x4*)(part + PS + e), p2 = *(const f32x4*)(part + 2 * PS + e), p3 = *(const f32x4*)(part + 3 * PS + e);
    const u32x2 xb = *(const u32x2*)(x + e), bb = *(const u32x2*)(b2 + col);
    float r[4];
#pragma unroll
    for (int j = 0; j < 4; ++j) {
        const float xv = (j & 1) ? __uint_as_float(xb[j >> 1] & 0xffff0000u) : __uint_as_float(xb[j >> 1] << 16);
        const float bv = (j & 1) ? __uint_as_float(bb[j >> 1] & 0xffff0000u) : __uint_as_float(bb[j >> 1] << 16);
        const float s = rbf((p0[j] + p1[j]) + p2[j]);
        const float mm = rbf(rbf(p3[j]) + bv);
        const float g = rbf(mm * __builtin_amdgcn_rcpf(1.f + __builtin_amdgcn_exp2f(-mm * 1.4426950408889634f)));
        r[j] = xv + rbf(s * g);
    }
    u32x2 w; w[0] = cvtpk(r[0], r[1]); w[1] = cvtpk(r[2], r[3]);
    *(u32x2*)(out + e) = w;
}

extern "C" void kernel_launch(void* const* d_in, const int* in_sizes, int n_in, void* d_out, int out_size, void* d_ws, size_t ws_size, hipStream_t stream) {
    const u16* x  = (const u16*)d_in[0];
    const u16* kc = (const u16*)d_in[1];
    const u16* vc = (const u16*)d_in[2];
    const u16* wq = (const u16*)d_in[3];
    const u16* wo = (const u16*)d_in[4];
    const u16* w1 = (const u16*)d_in[6];
    const u16* b1 = (const u16*)d_in[7];
    const u16* w2 = (const u16*)d_in[8];
    const u16* b2 = (const u16*)d_in[9];
    char* ws = (char*)d_ws;
    u16* xn = (u16*)ws;
    u16* y  = (u16*)(ws + (size_t)4 * 1024 * 1024);
    u16* hb = (u16*)(ws + (size_t)8 * 1024 * 1024);
    u16* ob = (u16*)(ws + (size_t)16 * 1024 * 1024);
    float* part = (float*)(ws + (size_t)48 * 1024 * 1024);
    hipFuncSetAttribute((const void*)k1_attn, hipFuncAttributeMaxDynamicSharedMemorySize, K1_LDS);
    hipFuncSetAttribute((const void*)k2_gemm, hipFuncAttributeMaxDynamicSharedMemorySize, K2_LDS);
    hipLaunchKernelGGL(k0_ln, dim3(SEQ / 4), dim3(256), 0, stream, x, xn, y);
    hipLaunchKernelGGL(k1_attn, dim3(256), dim3(512), K1_LDS, stream, xn, y, kc, vc, wq, w1, b1, ob, hb);
    hipLaunchKernelGGL(k2_gemm, dim3(256), dim3(512), K2_LDS, stream, ob, hb, wo, w2, part);
    hipLaunchKernelGGL(k3_final, dim3(SEQ * DM / 4 / 256), dim3(256), 0, stream, part, x, b2, (u16*)d_out);
}
```

```cpp
#include <hip/hip_runtime.h>
#include <hip/hip_bf16.h>
#include <stdint.h>

using bf16x8 = __attribute__((ext_vector_type(8))) short;
using s16x4  = __attribute__((ext_vector_type(4))) short;
using f32x16 = __attribute__((ext_vector_type(16))) float;
using f32x4  = __attribute__((ext_vector_type(4))) float;
using u32x4  = __attribute__((ext_vector_type(4))) unsigned;
using u32x2  = __attribute__((ext_vector_type(2))) unsigned;
typedef short v4i16_t __attribute__((ext_vector_type(4)));
typedef __attribute__((address_space(3))) const char* lds_cptr;
typedef unsigned short u16;

constexpr int SEQ = 2048, DM = 640, NH = 32, DH = 192, DQ = 6144, FF = 1536;

#define DI __device__ __forceinline__
#define SBAR() __builtin_amdgcn_sched_barrier(0)
#define PIN(x) asm volatile("" : "+v"(x))
#define VAL(x) x
#define MFMA(a, b, c) __builtin_amdgcn_mfma_f32_32x32x16_bf16(a, b, c, 0, 0, 0)
#define WAIT_BAR(N) asm volatile("s_waitcnt vmcnt(" #N ") lgkmcnt(0)\n\ts_barrier" ::: "memory")

DI float bf2f(u16 b) { return __uint_as_float((unsigned)b << 16); }
typedef float f32x2 __attribute__((ext_vector_type(2)));
typedef __bf16 bf16x2_t __attribute__((ext_vector_type(2)));
DI unsigned cvtpk(float lo, float hi) { f32x2 v = {lo, hi}; bf16x2_t b = __builtin_convertvector(v, bf16x2_t); return __builtin_bit_cast(unsigned, b); }
DI float rbf(float x) { return __uint_as_float(cvtpk(x, x) << 16); }
DI void glds16(const void* g, unsigned lds_base) {
    unsigned sv;
    asm volatile("s_mov_b32 %0, m0\n\ts_mov_b32 m0, %2\n\ts_nop 0\n\tglobal_load_lds_dwordx4 %1, off\n\ts_mov_b32 m0, %0" : "=&s"(sv) : "v"(g), "s"(lds_base) : "memory");
}
DI void gldss(unsigned voff, const void* sbase, unsigned lds_base) {
    unsigned sv;
    asm volatile("s_mov_b32 %0, m0\n\ts_mov_b32 m0, %3\n\ts_nop 0\n\tglobal_load_lds_dwordx4 %1, %2\n\ts_mov_b32 m0, %0" : "=&s"(sv) : "v"(voff), "s"(sbase), "s"(lds_base) : "memory");
}
DI bf16x8 ldsr(lds_cptr p) { return *(const __attribute__((address_space(3))) bf16x8*)p; }
DI s16x4 vtr(lds_cptr p) { return __builtin_bit_cast(s16x4, __builtin_amdgcn_ds_read_tr16_b64_v4i16((__attribute__((address_space(3))) v4i16_t*)p)); }
DI bf16x8 cat(s16x4 a, s16x4 b) { return __builtin_shufflevector(a, b, 0, 1, 2, 3, 4, 5, 6, 7); }
DI unsigned rfl(unsigned x) { return (unsigned)__builtin_amdgcn_readfirstlane((int)x); }
DI float xhalf_max(float m) { auto rr = __builtin_amdgcn_permlane32_swap(__float_as_uint(m), __float_as_uint(m), false, false); return __builtin_fmaxf(__uint_as_float(rr[0]), __uint_as_float(rr[1])); }
DI float xhalf_sum(float m) { auto rr = __builtin_amdgcn_permlane32_swap(__float_as_uint(m), __float_as_uint(m), false, false); return __uint_as_float(rr[0]) + __uint_as_float(rr[1]); }

DI float wave_sum(float v) {
#pragma unroll
    for (int o = 32; o > 0; o >>= 1) v += __shfl_xor(v, o);
    return v;
}
constexpr int K0_LN = SEQ / 4, K0_WS = FF / 64;
__global__ __launch_bounds__(256) void k0_ln(const u16* __restrict__ x, const u16* __restrict__ w1, u16* __restrict__ xn, float* __restrict__ st2, float* __restrict__ w1s) {
    const int lane = threadIdx.x & 63;
    if (blockIdx.x >= SEQ / 4) {
        __shared__ float red[32][65];
        const int cg = threadIdx.x & 7, rg = threadIdx.x >> 3, col0 = (blockIdx.x - SEQ / 4) * 64;
        const u32x4* src = (const u32x4*)(w1 + (size_t)(rg * 20) * FF + col0 + cg * 8);
        u32x4 t[20];
#pragma unroll
        for (int i = 0; i < 20; ++i) t[i] = src[(size_t)i * (FF / 8)];
        float a[8] = {0.f, 0.f, 0.f, 0.f, 0.f, 0.f, 0.f, 0.f};
#pragma unroll
        for (int i = 0; i < 20; ++i)
#pragma unroll
            for (int e = 0; e < 4; ++e) { a[2 * e] += __uint_as_float(t[i][e] << 16); a[2 * e + 1] += __uint_as_float(t[i][e] & 0xffff0000u); }
#pragma unroll
        for (int e = 0; e < 8; ++e) red[rg][cg * 8 + e] = a[e];
        __syncthreads();
        if (threadIdx.x < 64) { float s = 0.f;
#pragma unroll
            for (int r = 0; r < 32; ++r) s += red[r][threadIdx.x];
            w1s[col0 + threadIdx.x] = s; }
        return;
    }
    const int row = blockIdx.x * 4 + (threadIdx.x >> 6);
    const u32x4* xr = (const u32x4*)(x + (size_t)row * DM);
    const bool two = lane < 16;
    u32x4 w0 = xr[lane], w1v = two ? xr[64 + lane] : (u32x4){0u, 0u, 0u, 0u};
    float v[16];
#pragma unroll
    for (int i = 0; i < 4; ++i) { v[2 * i] = __uint_as_float(w0[i] << 16); v[2 * i + 1] = __uint_as_float(w0[i] & 0xffff0000u);
                                  v[8 + 2 * i] = __uint_as_float(w1v[i] << 16); v[8 + 2 * i + 1] = __uint_as_float(w1v[i] & 0xffff0000u); }
    const size_t base = (size_t)(row >> 5) * 40 * 64 * 8 + (size_t)(row & 31) * 8;
    const size_t off0 = base + (size_t)(lane >> 1) * 512 + (lane & 1) * 256;
    const size_t off1 = base + (size_t)((64 + lane) >> 1) * 512 + (lane & 1) * 256;
#pragma unroll
    for (int pass = 0; pass < 2; ++pass) {
        float s = 0.f;
#pragma unroll
        for (int i = 0; i < 16; ++i) s += v[i];
        const float mean = wave_sum(s) * (1.0f / DM);
        float q = 0.f;
#pragma unroll
        for (int i = 0; i < 8; ++i) { float d = v[i] - mean; q += d * d; }
        if (two) {
#pragma unroll
            for (int i = 8; i < 16; ++i) { float d = v[i] - mean; q += d * d; }
        }
        const float var = wave_sum(q) * (1.0f / DM);
        const float mb = rbf(mean), vb = rbf(var);
        const float den = rbf(__fsqrt_rn(rbf(vb + 1e-5f)));
        if (pass == 1) { if (lane == 0) { st2[2 * row] = mb; st2[2 * row + 1] = den; } break; }
#pragma unroll
        for (int i = 0; i < 16; ++i) v[i] = rbf(__fdiv_rn(rbf(v[i] - mb), den));
        if (!two) {
#pragma unroll
            for (int i = 8; i < 16; ++i) v[i] = 0.f;
        }
        u32x4 a, b;
#pragma unroll
        for (int i = 0; i < 4; ++i) { a[i] = (__float_as_uint(v[2 * i]) >> 16) | (__float_as_uint(v[2 * i + 1]) & 0xffff0000u);
                                      b[i] = (__float_as_uint(v[8 + 2 * i]) >> 16) | (__float_as_uint(v[8 + 2 * i + 1]) & 0xffff0000u); }
        *(u32x4*)(xn + off0) = a;
        if (two) *(u32x4*)(xn + off1) = b;
    }
}

constexpr int SLOT = 24576, LDS_K = 0, LDS_V = 3 * SLOT, PSLOT = 32768, K1_LDS = 163840;
constexpr float CSC = 0.07216878364870322f * 1.4426950408889634f;
constexpr float THR = 8.f;

DI float gelu_tanh(float x) {
    const float u = 0.7978845608028654f * (x + 0.044715f * x * x * x);
    const float e = __builtin_amdgcn_exp2f(u * 2.8853900817779268f);
    const float t = 1.f - 2.f * __builtin_amdgcn_rcpf(e + 1.f);
    return 0.5f * x * (1.f + t);
}
template <int OFF> DI void gld16(bf16x8& d, unsigned voff, const void* sb) { asm volatile("global_load_dwordx4 %0, %1, %2 offset:%3" : "=v"(d) : "v"(voff), "s"(sb), "i"(OFF) : "memory"); }
template <int OFF> DI void gld16f(f32x4& d, unsigned voff, const void* sb) { asm volatile("global_load_dwordx4 %0, %1, %2 offset:%3" : "=v"(d) : "v"(voff), "s"(sb), "i"(OFF) : "memory"); }
template <int OFF> DI void gld8(u32x2& d, unsigned voff, const void* sb) { asm volatile("global_load_dwordx2 %0, %1, %2 offset:%3" : "=v"(d) : "v"(voff), "s"(sb), "i"(OFF) : "memory"); }

template <int I> DI bf16x8 pfrag(lds_cptr wqp, lds_cptr w1p) {
    constexpr int s = I >> 3, j = I & 7;
    if constexpr (j < 6) { constexpr int off = 2 * s * 3072 + (j >> 1) * 1024 + (j & 1) * 512; return cat(vtr(wqp + off), vtr(wqp + off + 256)); }
    else                 { constexpr int off = 2 * s * 1024 + (j - 6) * 512; return cat(vtr(w1p + off), vtr(w1p + off + 256)); }
}
template <int I> DI void pstep(f32x16 (&o6)[6], f32x16 (&hacc)[2], bf16x8 (&f)[3], const bf16x8 (&xf)[4], lds_cptr wqp, lds_cptr w1p) {
    constexpr int s = I >> 3, j = I & 7;
    if constexpr (I + 2 < 32) f[(I + 2) % 3] = pfrag<I + 2>(wqp, w1p);
    if constexpr (j < 6) o6[j] = MFMA(f[I % 3], xf[s], o6[j]); else hacc[j - 6] = MFMA(f[I % 3], xf[s], hacc[j - 6]);
    SBAR();
    if constexpr (I + 1 < 32) pstep<I + 1>(o6, hacc, f, xf, wqp, w1p);
}

__global__ __launch_bounds__(512, 2)
void k1_attn(const u16* __restrict__ xn, const float* __restrict__ st2, const float* __restrict__ w1sum, const u16* __restrict__ Kc, const u16* __restrict__ Vc,
             const u16* __restrict__ wq, const u16* __restrict__ w1, const u16* __restrict__ b1,
             u16* __restrict__ o, u16* __restrict__ hbuf, const u16* __restrict__ wo, const u16* __restrict__ w2, u16* __restrict__ wpk
             ) {
    extern __shared__ __attribute__((aligned(16))) char lds[];
    const int tid = threadIdx.x, lane = tid & 63, r32 = lane & 31, hi = lane >> 5;
#define STAMP(i)
    STAMP(0);
    const int wid = __builtin_amdgcn_readfirstlane(tid >> 6);
    const int bid = blockIdx.x, xcd = bid & 7, jj = bid >> 3;
    const int head = xcd + 8 * (jj >> 3), qb = jj & 7;
    const int row0 = qb * 256 + wid * 32;
    const unsigned lds0 = (unsigned)(uintptr_t)lds;
    const lds_cptr L = (lds_cptr)lds;

    const int vd1 = lane >> 5, vr7 = (lane >> 2) & 7, vch = lane & 3;
    const int kr7 = ((lane >> 4) << 1) | (((lane >> 3) & 1) ^ ((wid >> 1) & 1)), kc8 = (lane & 7) ^ kr7;
    const int q4 = (lane & 15) >> 2, p4 = lane & 3, g1 = (lane >> 4) & 1;
    const int r7 = r32 & 7;

    const char* kbase = (const char*)(Kc + (size_t)head * SEQ * DH);
    const char* vbase = (const char*)(Vc + (size_t)head * SEQ * DH);
    const unsigned kvo = kr7 * (DH * 2) + kc8 * 16, vvo = vr7 * (DH * 2) + vd1 * 64 + vch * 16;
    const unsigned kdst = lds0 + LDS_K + wid * 3072, vdst = lds0 + LDS_V + wid * 3072;
#define DMA_K(t, slot) do { const char* s_ = kbase + (size_t)((64 * (t) + 32 + 8 * wid) & (SEQ - 1)) * (DH * 2); \
        gldss(kvo, s_, kdst + (slot)); gldss(kvo, s_ + 128, kdst + (slot) + 1024); gldss(kvo, s_ + 256, kdst + (slot) + 2048); } while (0)
#define DMA_V(t, slot) do { const char* s_ = vbase + (size_t)((64 * (t) + 8 * wid) & (SEQ - 1)) * (DH * 2); \
        gldss(vvo, s_, vdst + (slot)); gldss(vvo, s_ + 128, vdst + (slot) + 1024); gldss(vvo, s_ + 256, vdst + (slot) + 2048); } while (0)

    f32x16 o6[6];
    f32x16 hacc[2];
#pragma unroll
    for (int i = 0; i < 6; ++i) o6[i] = f32x16{};
    hacc[0] = f32x16{}; hacc[1] = f32x16{};
    const int fcol0 = (48 * head < FF - 64) ? 48 * head : FF - 64;
    u32x2 bb[8], stv; f32x4 wsv[8];
    {
        const char* wqs = (const char*)(wq + (size_t)(8 * wid) * DQ + head * DH);
        const char* w1s = (const char*)(w1 + (size_t)(8 * wid) * FF + fcol0);
        const unsigned wqo = vr7 * (DQ * 2) + vd1 * 64 + vch * 16, w1o = vr7 * (FF * 2) + vd1 * 64 + vch * 16;
        const unsigned wqd = lds0 + wid * 3072, w1d = lds0 + 24576 + wid * 1024;
#define CSRC(c) (c)
#define DMA_W(c) do { \
            gldss(wqo, wqs + (size_t)(c) * 64 * DQ * 2,       wqd + ((c) % 5) * PSLOT); \
            gldss(wqo, wqs + (size_t)(c) * 64 * DQ * 2 + 128, wqd + ((c) % 5) * PSLOT + 1024); \
            gldss(wqo, wqs + (size_t)(c) * 64 * DQ * 2 + 256, wqd + ((c) % 5) * PSLOT + 2048); \
            gldss(w1o, w1s + (size_t)(c) * 64 * FF * 2,       w1d + ((c) % 5) * PSLOT); } while (0)
        const int p4s = ((p4 & 1) << 1) | (p4 >> 1);
        const lds_cptr wqp = L + hi * 3072 + q4 * 64 + g1 * 32 + p4s * 8;
        const lds_cptr w1p = L + 24576 + hi * 1024 + q4 * 64 + g1 * 32 + p4 * 8;
        const char* xsrc = (const char*)(xn + (size_t)(row0 >> 5) * 40 * 512);
        const char* bsrc = (const char*)(b1 + fcol0);
        const char* wssrc = (const char*)(w1sum + fcol0);
        const char* stsrc = (const char*)(st2 + 2 * row0);
        const unsigned xo = lane * 16, bo = 8 * hi, wso = 16 * hi, sto = 8 * r32;
        bf16x8 xa[4], xb[4], f[3];
#define LDX(c, X) do { const char* xp_ = xsrc + CSRC(c) * 4096; \
            gld16<0>(X[0], xo, xp_); gld16<1024>(X[1], xo, xp_); gld16<2048>(X[2], xo, xp_); gld16<3072>(X[3], xo, xp_); } while (0)
#define WAITX(N, X) asm volatile("s_waitcnt vmcnt(" #N ") lgkmcnt(0)\n\ts_barrier" : "+v"(X[0]), "+v"(X[1]), "+v"(X[2]), "+v"(X[3]) :: "memory")
#define PCOMP(c, X) do { lds_cptr a_ = wqp + ((c) % 5) * PSLOT; lds_cptr b_ = w1p + ((c) % 5) * PSLOT; PIN(a_); PIN(b_); \
            f[0] = pfrag<0>(a_, b_); f[1] = pfrag<1>(a_, b_); SBAR(); pstep<0>(o6, hacc, f, X, a_, b_); } while (0)
        LDX(0, xa); DMA_W(0); DMA_W(1); DMA_W(2); DMA_W(3);
        WAITX(12, xa); LDX(1, xb); DMA_W(4);                          PCOMP(0, xa);
        WAITX(4, xb);  LDX(2, xa); DMA_W(5);                          PCOMP(1, xb);
        WAITX(4, xa);  LDX(3, xb); DMA_W(6);                          PCOMP(2, xa);
        WAITX(4, xb);  LDX(4, xa); DMA_W(7);                          PCOMP(3, xb);
        WAITX(4, xa);  LDX(5, xb); DMA_W(8);                          PCOMP(4, xa);
        WAITX(4, xb);  LDX(6, xa); DMA_W(9);                          PCOMP(5, xb);
        WAITX(4, xa);  LDX(7, xb); DMA_K(-1, 0);                      PCOMP(6, xa);
        WAITX(3, xb);  LDX(8, xa); DMA_K(0, SLOT);                    PCOMP(7, xb);
        WAITX(3, xa);  LDX(9, xb); DMA_K(1, 2 * SLOT); DMA_V(0, 0);   PCOMP(8, xa);
        WAITX(6, xb);
        gld8<0>(bb[0], bo, bsrc); gld8<16>(bb[1], bo, bsrc); gld8<32>(bb[2], bo, bsrc); gld8<48>(bb[3], bo, bsrc);
        gld8<64>(bb[4], bo, bsrc); gld8<80>(bb[5], bo, bsrc); gld8<96>(bb[6], bo, bsrc); gld8<112>(bb[7], bo, bsrc);
        gld16f<0>(wsv[0], wso, wssrc); gld16f<32>(wsv[1], wso, wssrc); gld16f<64>(wsv[2], wso, wssrc); gld16f<96>(wsv[3], wso, wssrc);
        gld16f<128>(wsv[4], wso, wssrc); gld16f<160>(wsv[5], wso, wssrc); gld16f<192>(wsv[6], wso, wssrc); gld16f<224>(wsv[7], wso, wssrc);
        gld8<0>(stv, sto, stsrc);
        DMA_V(1, SLOT);                                               PCOMP(9, xb);
        asm volatile("s_waitcnt vmcnt(3)" : "+v"(bb[0]), "+v"(bb[1]), "+v"(bb[2]), "+v"(bb[3]), "+v"(bb[4]), "+v"(bb[5]), "+v"(bb[6]), "+v"(bb[7]),
                     "+v"(wsv[0]), "+v"(wsv[1]), "+v"(wsv[2]), "+v"(wsv[3]), "+v"(wsv[4]), "+v"(wsv[5]), "+v"(wsv[6]), "+v"(wsv[7]), "+v"(stv) :: "memory");
#undef DMA_W
#undef LDX
#undef WAITX
#undef PCOMP
    }
    STAMP(1);
    bf16x8 qf[12];
#pragma unroll
    for (int db = 0; db < 6; ++db)
#pragma unroll
        for (int s = 0; s < 2; ++s) {
            u32x4 w;
#pragma unroll
            for (int e = 0; e < 4; ++e) w[e] = cvtpk(o6[db][8 * s + 2 * e], o6[db][8 * s + 2 * e + 1]);
            qf[2 * db + s] = __builtin_bit_cast(bf16x8, w);
        }
    {
        const int flo = 48 * head - fcol0;
        const float m2 = __uint_as_float(stv[0]), rden = __fdiv_rn(1.0f, __uint_as_float(stv[1]));
        char* hbase = (char*)hbuf + (size_t)(row0 >> 5) * 96 * 1024 + r32 * 16 + 8 * hi;
#pragma unroll
        for (int ft = 0; ft < 2; ++ft)
#pragma unroll
            for (int g = 0; g < 4; ++g) {
                const int fl = 32 * ft + 8 * g;
                float hv[4];
#pragma unroll
                for (int e = 0; e < 4; ++e) {
                    const unsigned bw = bb[4 * ft + g][e >> 1];
                    const float bias = (e & 1) ? __uint_as_float(bw & 0xffff0000u) : __uint_as_float(bw << 16);
                    hv[e] = gelu_tanh(rbf(rbf((hacc[ft][4 * g + e] - m2 * wsv[4 * ft + g][e]) * rden) + bias));
                }
                u32x2 w; w[0] = cvtpk(hv[0], hv[1]); w[1] = cvtpk(hv[2], hv[3]);
                const int fc = (fcol0 + fl) >> 3;
                if (fl >= flo && fl < flo + 48) *(u32x2*)(hbase + (fc >> 1) * 1024 + (fc & 1) * 512) = w;
            }
    }
#pragma unroll
    for (int i = 0; i < 6; ++i) o6[i] = f32x16{};

    lds_cptr kp[4];
#pragma unroll
    for (int j = 0; j < 4; ++j)
        kp[j] = L + LDS_K + (r32 >> 3) * 3072 + ((r7 >> 1) << 8) + ((((r32 >> 4) & 1) ^ (r7 & 1)) << 7) + ((r7 ^ (2 * j + hi)) << 4);
    const lds_cptr vp = L + LDS_V + (4 * hi + q4) * 64 + g1 * 32 + p4 * 8;

    f32x16 SA, SB; float mref, lsum = 0.f;
    u32x4 P0, P1;
#define KRD(sl_, half, ks) ldsr(kp[(ks) & 3] + (sl_) + (half) * 12288 + ((ks) >> 2) * 1024)
#define VRD(sl_, half, s, db) cat(vtr(vp + (sl_) + (4 * (half) + 2 * (s)) * 3072 + ((db) >> 1) * 1024 + ((db) & 1) * 512), \
                                  vtr(vp + (sl_) + (4 * (half) + 2 * (s) + 1) * 3072 + ((db) >> 1) * 1024 + ((db) & 1) * 512))
    {
        SA = f32x16{};
#pragma unroll
        for (int ks = 0; ks < 12; ++ks) SA = MFMA(KRD(0, 1, ks), qf[ks], SA);
        float mx = SA[0];
#pragma unroll
        for (int i = 1; i < 16; ++i) mx = __builtin_fmaxf(mx, SA[i]);
        mref = xhalf_max(mx) * CSC;
    }
    WAIT_BAR(3);

#define EX(v) __builtin_amdgcn_exp2f(__builtin_fmaf((v), CSC, nm))
#define MX3(a, b, c) __builtin_fmaxf(__builtin_fmaxf((a), (b)), (c))
#define GEND() do { asm volatile("" ::: "memory"); SBAR(); } while (0)
#define HSTEP(SC, SN, ksl, khalf, vsl, vhalf, nksl, nkhalf, D0, D1, D2, D3, D4, D5) do { \
        const float nm = -mref; float sa, sb, mx; \
        k3 = KRD(ksl, khalf, 3);  SN = MFMA(k0, qf[0], f32x16{}); PIN(SN); float p0 = EX(SC[0]); PIN(p0); GEND(); \
        k0 = KRD(ksl, khalf, 4);  SN = MFMA(k1, qf[1], SN); PIN(SN); D0; float p1 = EX(SC[1]); PIN(p1); GEND(); \
        k1 = KRD(ksl, khalf, 5);  SN = MFMA(k2, qf[2], SN); PIN(SN); float p2 = EX(SC[2]); P0[0] = cvtpk(p0, p1); PIN(p2); PIN(P0); GEND(); \
        k2 = KRD(ksl, khalf, 6);  SN = MFMA(k3, qf[3], SN); PIN(SN); float p3 = EX(SC[3]); sa = p0 + p1; PIN(p3); PIN(sa); GEND(); \
        k3 = KRD(ksl, khalf, 7);  SN = MFMA(k0, qf[4], SN); PIN(SN); float p4 = EX(SC[4]); P0[1] = cvtpk(p2, p3); PIN(p4); PIN(P0); GEND(); \
        k0 = KRD(ksl, khalf, 8);  SN = MFMA(k1, qf[5], SN); PIN(SN); D1; float p5 = EX(SC[5]); sb = p2 + p3; PIN(p5); PIN(sb); GEND(); \
        k1 = KRD(ksl, khalf, 9);  SN = MFMA(k2, qf[6], SN); PIN(SN); float p6 = EX(SC[6]); P0[2] = cvtpk(p4, p5); PIN(p6); PIN(P0); GEND(); \
        k2 = KRD(ksl, khalf, 10); SN = MFMA(k3, qf[7], SN); PIN(SN); float p7 = EX(SC[7]); sa += p4; sb += p5; PIN(p7); PIN(sa); PIN(sb); GEND(); \
        k3 = KRD(ksl, khalf, 11); SN = MFMA(k0, qf[8], SN); PIN(SN); float p8 = EX(SC[8]); P0[3] = cvtpk(p6, p7); PIN(p8); PIN(P0); GEND(); \
        bf16x8 v0 = VRD(vsl, vhalf, 0, 0); SN = MFMA(k1, qf[9], SN); PIN(SN); D2; float p9 = EX(SC[9]); sa += p6; sb += p7; PIN(p9); PIN(sa); PIN(sb); GEND(); \
        bf16x8 v1 = VRD(vsl, vhalf, 0, 1); SN = MFMA(k2, qf[10], SN); PIN(SN); float p10 = EX(SC[10]); P1[0] = cvtpk(p8, p9); PIN(p10); PIN(P1); GEND(); \
        bf16x8 v2 = VRD(vsl, vhalf, 0, 2); SN = MFMA(k3, qf[11], SN); PIN(SN); float p11 = EX(SC[11]); sa += p8; sb += p9; PIN(p11); PIN(sa); PIN(sb); GEND(); \
        const bf16x8 pa0 = __builtin_bit_cast(bf16x8, P0); \
        bf16x8 v3; \
        v3 = VRD(vsl, vhalf, 0, 3); o6[0] = MFMA(v0, pa0, o6[0]); PIN(o6[0]); float p12 = EX(SC[12]); P1[1] = cvtpk(p10, p11); PIN(p12); PIN(P1); GEND(); \
        v0 = VRD(vsl, vhalf, 0, 4); o6[1] = MFMA(v1, pa0, o6[1]); PIN(o6[1]); D3; float p13 = EX(SC[13]); sa += p10; sb += p11; PIN(p13); PIN(sa); PIN(sb); GEND(); \
        v1 = VRD(vsl, vhalf, 0, 5); o6[2] = MFMA(v2, pa0, o6[2]); PIN(o6[2]); float p14 = EX(SC[14]); P1[2] = cvtpk(p12, p13); PIN(p14); PIN(P1); GEND(); \
        v2 = VRD(vsl, vhalf, 1, 0); o6[3] = MFMA(v3, pa0, o6[3]); PIN(o6[3]); float p15 = EX(SC[15]); sa += p12; sb += p13; PIN(p15); PIN(sa); PIN(sb); GEND(); \
        v3 = VRD(vsl, vhalf, 1, 1); o6[4] = MFMA(v0, pa0, o6[4]); PIN(o6[4]); P1[3] = cvtpk(p14, p15); mx = MX3(SN[0], SN[1], SN[2]); mx = MX3(mx, SN[3], SN[4]); PIN(P1); PIN(mx); GEND(); \
        v0 = VRD(vsl, vhalf, 1, 2); o6[5] = MFMA(v1, pa0, o6[5]); PIN(o6[5]); D4; sa += p14; sb += p15; mx = MX3(mx, SN[5], SN[6]); PIN(sa); PIN(sb); PIN(mx); GEND(); \
        const bf16x8 pa1 = __builtin_bit_cast(bf16x8, P1); \
        v1 = VRD(vsl, vhalf, 1, 3); o6[0] = MFMA(v2, pa1, o6[0]); PIN(o6[0]); mx = MX3(mx, SN[7], SN[8]); lsum += sa + sb; PIN(mx); PIN(lsum); GEND(); \
        v2 = VRD(vsl, vhalf, 1, 4); o6[1] = MFMA(v3, pa1, o6[1]); PIN(o6[1]); mx = MX3(mx, SN[9], SN[10]); mx = MX3(mx, SN[11], SN[12]); PIN(mx); GEND(); \
        v3 = VRD(vsl, vhalf, 1, 5); o6[2] = MFMA(v0, pa1, o6[2]); PIN(o6[2]); mx = MX3(mx, SN[13], SN[14]); mx = __builtin_fmaxf(mx, SN[15]); PIN(mx); GEND(); \
        k0 = KRD(nksl, nkhalf, 0);  o6[3] = MFMA(v1, pa1, o6[3]); PIN(o6[3]); D5; mx = xhalf_max(mx); PIN(mx); GEND(); \
        k1 = KRD(nksl, nkhalf, 1);  o6[4] = MFMA(v2, pa1, o6[4]); PIN(o6[4]); mx = __builtin_fmaf(mx, CSC, nm); PIN(mx); GEND(); \
        k2 = KRD(nksl, nkhalf, 2);  o6[5] = MFMA(v3, pa1, o6[5]); PIN(o6[5]); GEND(); \
        if (__builtin_expect(__any(mx > THR), 0)) { \
            const float dl = __builtin_fmaxf(mx, 0.f); mref += dl; const float f = __builtin_amdgcn_exp2f(-dl); lsum *= f; \
            _Pragma("unroll") for (int d_ = 0; d_ < 6; ++d_) _Pragma("unroll") for (int r_ = 0; r_ < 16; ++r_) o6[d_][r_] *= f; \
        } \
    } while (0)

    bf16x8 k0 = KRD(SLOT, 0, 0), k1 = KRD(SLOT, 0, 1), k2 = KRD(SLOT, 0, 2), k3;
    u32x4 pkv = {0u, 0u, 0u, 0u};
    const int pk0 = (bid * 8 + wid) * 5;
    const unsigned pko = vr7 * (DM * 2) + vd1 * 64 + vch * 16, xo16 = lane * 16;
    STAMP(2);
    int s0 = 0;
    for (int t = 0; t < 32; ++t) {
        const int s1 = (s0 == 2 * SLOT) ? 0 : s0 + SLOT;
        const int s2 = (s0 == 0) ? 2 * SLOT : s0 - SLOT;
        if (t >= 1 && t <= 5 && pk0 + t - 1 < 9600) asm volatile("global_store_dwordx4 %0, %1, %2\n\ts_nop 1" :: "v"(xo16), "v"(pkv), "s"((const char*)wpk + (size_t)(pk0 + t - 1) * 1024) : "memory");
        if (t < 5 && pk0 + t < 9600) {
            const int P = pk0 + t, b = P >> 4, tn = b / 120, g = b - tn * 120;
            const char* src = ((g < 96) ? (const char*)(wo + (size_t)g * 64 * DM) : (const char*)(w2 + (size_t)(g - 96) * 64 * DM)) + (size_t)(8 * ((P >> 1) & 7)) * (DM * 2) + tn * 256 + (P & 1) * 128;
            asm volatile("global_load_dwordx4 %0, %1, %2" : "+v"(pkv) : "v"(pko), "s"(src) : "memory");
        }
        const char* ks_ = kbase + (size_t)((64 * (t + 2) + 32 + 8 * wid) & (SEQ - 1)) * (DH * 2);
        const char* vs_ = vbase + (size_t)((64 * (t + 2) + 8 * wid) & (SEQ - 1)) * (DH * 2);
        HSTEP(SA, SB, s1, 0, s0, 0, s1, 1, gldss(kvo, ks_, kdst + s0), gldss(kvo, ks_ + 128, kdst + s0 + 1024), gldss(kvo, ks_ + 256, kdst + s0 + 2048),
              gldss(vvo, vs_, vdst + s2), gldss(vvo, vs_ + 128, vdst + s2 + 1024), gldss(vvo, vs_ + 256, vdst + s2 + 2048));
        HSTEP(SB, SA, s1, 1, s0, 1, s2, 0, (void)0, (void)0, (void)0, (void)0, (void)0, (void)0);
        WAIT_BAR(3);
        s0 = s1;
    }
    STAMP(3);
    asm volatile("s_waitcnt vmcnt(0)" ::: "memory");
    {
        const float il = __builtin_amdgcn_rcpf(xhalf_sum(lsum));
        char* obase = (char*)o + ((size_t)(row0 >> 5) * 384 + head * 12) * 1024 + r32 * 16 + 8 * hi;
#pragma unroll
        for (int db = 0; db < 6; ++db)
#pragma unroll
            for (int g = 0; g < 4; ++g) {
                u32x2 w; w[0] = cvtpk(o6[db][4 * g] * il, o6[db][4 * g + 1] * il); w[1] = cvtpk(o6[db][4 * g + 2] * il, o6[db][4 * g + 3] * il);
                *(u32x2*)(obase + (2 * db + (g >> 1)) * 1024 + (g & 1) * 512) = w;
            }
    }
    STAMP(4);
}

constexpr int K2_STAGE = 16384, K2_LDS = 4 * K2_STAGE;
__global__ __launch_bounds__(512, 2)
void k2_gemm(const u16* __restrict__ o, const u16* __restrict__ hbuf, const u16* __restrict__ wpk, float* __restrict__ part
             ) {
    extern __shared__ __attribute__((aligned(16))) char lds[];
    const int tid = threadIdx.x, lane = tid & 63, r32 = lane & 31, hi = lane >> 5;
#define STAMP2(i)
    STAMP2(0);
    const int wid = __builtin_amdgcn_readfirstlane(tid >> 6);
    const int bid = blockIdx.x;
    const int lin = (bid & 7) * 30 + (bid >> 3);
    const int chunk = lin / 40, rem = lin - chunk * 40, tm = rem / 5, tn = rem - tm * 5;
    const unsigned lds0 = (unsigned)(uintptr_t)lds;
    const lds_cptr L = (lds_cptr)lds;
    const char* Ao = (const char*)o + (size_t)(8 * tm + wid) * 384 * 1024;
    const char* Ah = (const char*)hbuf + (size_t)(8 * tm + wid) * 96 * 1024;
    const char* Wp = (const char*)wpk + (size_t)tn * 120 * 16384 + wid * 2048;
    const unsigned avo = lane * 16, wvo = lane * 16;
    const unsigned wdst = lds0 + wid * 2048;
    const int g0 = chunk * 20;
    const int q4 = (lane & 15) >> 2, p4 = lane & 3, g1 = (lane >> 4) & 1;
    const lds_cptr wp = L + hi * 2048 + q4 * 64 + g1 * 32 + p4 * 8;
    f32x16 acc[4];
#pragma unroll
    for (int i = 0; i < 4; ++i) acc[i] = f32x16{};
    bf16x8 ab[4][4];
    f32x16 sv[4];
    auto flush = [&](const f32x16 (&ac)[4], int pbuf) {
        float* prow = part + ((size_t)pbuf * SEQ + tm * 256 + wid * 32 + r32) * DM + tn * 128 + 4 * hi;
#pragma unroll
        for (int ni = 0; ni < 4; ++ni)
#pragma unroll
            for (int g = 0; g < 4; ++g) { f32x4 v = {ac[ni][4 * g], ac[ni][4 * g + 1], ac[ni][4 * g + 2], ac[ni][4 * g + 3]}; *(f32x4*)(prow + 32 * ni + 8 * g) = v; }
    };
    const int rmod = (chunk == 4) ? 16 : 20;
#define K2ROT 0
    const int rot4 = K2ROT * ((tn + tm) % 5);
#define SRC2(st) int l_ = ((st) < 20) ? (st) : 19; if (l_ < rmod) { l_ += rot4; l_ = (l_ >= rmod) ? l_ - rmod : l_; } const int g_ = g0 + l_; const char* a_; const char* w_; \
        w_ = Wp + (size_t)g_ * 16384; if (g_ < 96) { a_ = Ao + (size_t)g_ * 4096; } else { a_ = Ah + (size_t)(g_ - 96) * 4096; }
#define ISSUE2(st, sl) do { SRC2(st); gld16<0>(ab[sl][0], avo, a_); gld16<1024>(ab[sl][1], avo, a_); gld16<2048>(ab[sl][2], avo, a_); gld16<3072>(ab[sl][3], avo, a_); \
        gldss(wvo, w_, wdst + (sl) * K2_STAGE); gldss(wvo, w_ + 1024, wdst + (sl) * K2_STAGE + 1024); } while (0)
    ISSUE2(0, 0); ISSUE2(1, 1); ISSUE2(2, 2);
#define WF(sl, s, ni) cat(vtr(wp + (sl) * K2_STAGE + (s) * 4096 + ((ni) >> 1) * 1024 + ((ni) & 1) * 512), vtr(wp + (sl) * K2_STAGE + (s) * 4096 + ((ni) >> 1) * 1024 + ((ni) & 1) * 512 + 256))
#define NS(sl) (((sl) + 3) & 3)
#define K2A(x) x
#define K2W(x) x
#define K2N "12"
#define STAGE2(st, sl) do { \
        asm volatile("s_waitcnt vmcnt(" K2N ") lgkmcnt(0)\n\ts_barrier" : "+v"(ab[sl][0]), "+v"(ab[sl][1]), "+v"(ab[sl][2]), "+v"(ab[sl][3]) :: "memory"); \
        SRC2((st) + 3); \
        bf16x8 f0 = WF(sl, 0, 0), f1 = WF(sl, 0, 1), f2 = WF(sl, 0, 2), f3; SBAR(); \
        f3 = WF(sl, 0, 3); acc[0] = MFMA(f0, ab[sl][0], acc[0]); K2A(gld16<0>(ab[NS(sl)][0], avo, a_)); SBAR(); \
        f0 = WF(sl, 1, 0); acc[1] = MFMA(f1, ab[sl][0], acc[1]); SBAR(); \
        f1 = WF(sl, 1, 1); acc[2] = MFMA(f2, ab[sl][0], acc[2]); K2A(gld16<1024>(ab[NS(sl)][1], avo, a_)); SBAR(); \
        f2 = WF(sl, 1, 2); acc[3] = MFMA(f3, ab[sl][0], acc[3]); SBAR(); \
        f3 = WF(sl, 1, 3); acc[0] = MFMA(f0, ab[sl][1], acc[0]); K2A(gld16<2048>(ab[NS(sl)][2], avo, a_)); SBAR(); \
        f0 = WF(sl, 2, 0); acc[1] = MFMA(f1, ab[sl][1], acc[1]); SBAR(); \
        f1 = WF(sl, 2, 1); acc[2] = MFMA(f2, ab[sl][1], acc[2]); K2A(gld16<3072>(ab[NS(sl)][3], avo, a_)); SBAR(); \
        f2 = WF(sl, 2, 2); acc[3] = MFMA(f3, ab[sl][1], acc[3]); SBAR(); \
        f3 = WF(sl, 2, 3); acc[0] = MFMA(f0, ab[sl][2], acc[0]); K2W(gldss(wvo, w_, wdst + NS(sl) * K2_STAGE)); SBAR(); \
        f0 = WF(sl, 3, 0); acc[1] = MFMA(f1, ab[sl][2], acc[1]); SBAR(); \
        f1 = WF(sl, 3, 1); acc[2] = MFMA(f2, ab[sl][2], acc[2]); K2W(gldss(wvo, w_ + 1024, wdst + NS(sl) * K2_STAGE + 1024)); SBAR(); \
        f2 = WF(sl, 3, 2); acc[3] = MFMA(f3, ab[sl][2], acc[3]); SBAR(); \
        f3 = WF(sl, 3, 3); acc[0] = MFMA(f0, ab[sl][3], acc[0]); SBAR(); \
        acc[1] = MFMA(f1, ab[sl][3], acc[1]); SBAR(); \
        acc[2] = MFMA(f2, ab[sl][3], acc[2]); SBAR(); \
        acc[3] = MFMA(f3, ab[sl][3], acc[3]); SBAR(); \
    } while (0)
    STAMP2(1);
    for (int s4 = 0; s4 < 16; s4 += 4) { STAGE2(s4, 0); STAGE2(s4 + 1, 1); STAGE2(s4 + 2, 2); STAGE2(s4 + 3, 3); }
#pragma unroll
    for (int i = 0; i < 4; ++i) { sv[i] = acc[i]; if (chunk == 4) acc[i] = f32x16{}; }
    STAGE2(16, 0); STAGE2(17, 1); STAGE2(18, 2); STAGE2(19, 3);
    STAMP2(2);
    asm volatile("s_waitcnt vmcnt(0)" : "+v"(ab[0][0]), "+v"(ab[0][1]), "+v"(ab[0][2]), "+v"(ab[0][3]), "+v"(ab[1][0]), "+v"(ab[1][1]), "+v"(ab[1][2]), "+v"(ab[1][3]),
                 "+v"(ab[2][0]), "+v"(ab[2][1]), "+v"(ab[2][2]), "+v"(ab[2][3]) :: "memory");
    flush(acc, chunk >= 4 ? chunk + 1 : chunk);
    if (chunk == 4) flush(sv, 4);
    STAMP2(3); STAMP2(4);
}

__global__ __launch_bounds__(256) void k3_final(const float* __restrict__ part, const u16* __restrict__ x, const u16* __restrict__ b2, u16* __restrict__ out) {
    const int i = blockIdx.x * 256 + threadIdx.x;
    const size_t e = (size_t)i * 4; const int col = (int)(e % DM);
    constexpr size_t PS = (size_t)SEQ * DM;
    f32x4 p[7];
#pragma unroll
    for (int j = 0; j < 7; ++j) p[j] = *(const f32x4*)(part + j * PS + e);
    const u32x2 xb = *(const u32x2*)(x + e), bb = *(const u32x2*)(b2 + col);
    float r[4];
#pragma unroll
    for (int j = 0; j < 4; ++j) {
        const float xv = (j & 1) ? __uint_as_float(xb[j >> 1] & 0xffff0000u) : __uint_as_float(xb[j >> 1] << 16);
        const float bv = (j & 1) ? __uint_as_float(bb[j >> 1] & 0xffff0000u) : __uint_as_float(bb[j >> 1] << 16);
        const float s = rbf(((p[0][j] + p[1][j]) + (p[2][j] + p[3][j])) + p[4][j]);
        const float mm = rbf(rbf(p[5][j] + p[6][j]) + bv);
        const float g = rbf(mm * __builtin_amdgcn_rcpf(1.f + __builtin_amdgcn_exp2f(-mm * 1.4426950408889634f)));
        r[j] = xv + rbf(s * g);
    }
    u32x2 w; w[0] = cvtpk(r[0], r[1]); w[1] = cvtpk(r[2], r[3]);
    *(u32x2*)(out + e) = w;
}

extern "C" void kernel_launch(void* const* d_in, const int* in_sizes, int n_in, void* d_out, int out_size, void* d_ws, size_t ws_size, hipStream_t stream) {
    const u16* x  = (const u16*)d_in[0];
    const u16* kc = (const u16*)d_in[1];
    const u16* vc = (const u16*)d_in[2];
    const u16* wq = (const u16*)d_in[3];
    const u16* wo = (const u16*)d_in[4];
    const u16* w1 = (const u16*)d_in[6];
    const u16* b1 = (const u16*)d_in[7];
    const u16* w2 = (const u16*)d_in[8];
    const u16* b2 = (const u16*)d_in[9];
    char* ws = (char*)d_ws;
    u16* xn = (u16*)ws;
    float* st2 = (float*)(ws + (size_t)4 * 1024 * 1024);
    float* w1s = (float*)(ws + (size_t)5 * 1024 * 1024);
    u16* hb = (u16*)(ws + (size_t)8 * 1024 * 1024);
    u16* ob = (u16*)(ws + (size_t)16 * 1024 * 1024);
    u16* wpk = (u16*)(ws + (size_t)96 * 1024 * 1024);
    float* part = (float*)(ws + (size_t)48 * 1024 * 1024);
    hipFuncSetAttribute((const void*)k1_attn, hipFuncAttributeMaxDynamicSharedMemorySize, K1_LDS);
    hipFuncSetAttribute((const void*)k2_gemm, hipFuncAttributeMaxDynamicSharedMemorySize, K2_LDS);
    hipLaunchKernelGGL(k0_ln, dim3(K0_LN + K0_WS), dim3(256), 0, stream, x, w1, xn, st2, w1s);
    hipLaunchKernelGGL(k1_attn, dim3(256), dim3(512), K1_LDS, stream, xn, st2, w1s, kc, vc, wq, w1, b1, ob, hb, wo, w2, wpk
        );
    hipLaunchKernelGGL(k2_gemm, dim3(240), dim3(512), K2_LDS, stream, ob, hb, wpk, part
        );
    hipLaunchKernelGGL(k3_final, dim3(SEQ * DM / 4 / 256), dim3(256), 0, stream, part, x, b2, (u16*)d_out);
}
```

```cpp
#include <hip/hip_runtime.h>
#include <hip/hip_bf16.h>
#include <stdint.h>
#include <type_traits>

using bf16x8 = __attribute__((ext_vector_type(8))) short;
using s16x4  = __attribute__((ext_vector_type(4))) short;
using f32x16 = __attribute__((ext_vector_type(16))) float;
using f32x4  = __attribute__((ext_vector_type(4))) float;
using u32x4  = __attribute__((ext_vector_type(4))) unsigned;
using u32x2  = __attribute__((ext_vector_type(2))) unsigned;
typedef short v4i16_t __attribute__((ext_vector_type(4)));
typedef __attribute__((address_space(3))) const char* lds_cptr;
typedef unsigned short u16;

constexpr int SEQ = 2048, DM = 640, NH = 32, DH = 192, DQ = 6144, FF = 1536;

#define DI __device__ __forceinline__
#define SBAR() __builtin_amdgcn_sched_barrier(0)
#define PIN(x) asm volatile("" : "+v"(x))
#define VAL(x) x
#define MFMA(a, b, c) __builtin_amdgcn_mfma_f32_32x32x16_bf16(a, b, c, 0, 0, 0)
#define WAIT_BAR(N) asm volatile("s_waitcnt vmcnt(" #N ") lgkmcnt(0)\n\ts_barrier" ::: "memory")

DI float bf2f(u16 b) { return __uint_as_float((unsigned)b << 16); }
typedef float f32x2 __attribute__((ext_vector_type(2)));
typedef __bf16 bf16x2_t __attribute__((ext_vector_type(2)));
DI unsigned cvtpk(float lo, float hi) { f32x2 v = {lo, hi}; bf16x2_t b = __builtin_convertvector(v, bf16x2_t); return __builtin_bit_cast(unsigned, b); }
DI float rbf(float x) { return __uint_as_float(cvtpk(x, x) << 16); }
DI void glds16(const void* g, unsigned lds_base) {
    unsigned sv;
    asm volatile("s_mov_b32 %0, m0\n\ts_mov_b32 m0, %2\n\ts_nop 0\n\tglobal_load_lds_dwordx4 %1, off\n\ts_mov_b32 m0, %0" : "=&s"(sv) : "v"(g), "s"(lds_base) : "memory");
}
DI void gldss(unsigned voff, const void* sbase, unsigned lds_base) {
    asm volatile("s_mov_b32 m0, %2\n\ts_nop 0\n\tglobal_load_lds_dwordx4 %0, %1" :: "v"(voff), "s"(sbase), "s"(lds_base) : "memory");
}
DI bf16x8 ldsr(lds_cptr p) { return *(const __attribute__((address_space(3))) bf16x8*)p; }
DI s16x4 vtr(lds_cptr p) { return __builtin_bit_cast(s16x4, __builtin_amdgcn_ds_read_tr16_b64_v4i16((__attribute__((address_space(3))) v4i16_t*)p)); }
DI bf16x8 cat(s16x4 a, s16x4 b) { return __builtin_shufflevector(a, b, 0, 1, 2, 3, 4, 5, 6, 7); }
DI unsigned rfl(unsigned x) { return (unsigned)__builtin_amdgcn_readfirstlane((int)x); }
DI float max3a(float a, float b, float c) { float r; asm("v_max3_f32 %0, %1, %2, %3" : "=v"(r) : "v"(a), "v"(b), "v"(c)); return r; }
DI float xhalf_max(float m) { auto rr = __builtin_amdgcn_permlane32_swap(__float_as_uint(m), __float_as_uint(m), false, false); return __builtin_fmaxf(__uint_as_float(rr[0]), __uint_as_float(rr[1])); }
DI float xhalf_sum(float m) { auto rr = __builtin_amdgcn_permlane32_swap(__float_as_uint(m), __float_as_uint(m), false, false); return __uint_as_float(rr[0]) + __uint_as_float(rr[1]); }

DI float wave_sum(float v) {
#pragma unroll
    for (int o = 32; o > 0; o >>= 1) v += __shfl_xor(v, o);
    return v;
}
constexpr int K0_LN = SEQ / 4, K0_WS = FF / 64;
__global__ __launch_bounds__(256) void k0_ln(const u16* __restrict__ x, const u16* __restrict__ w1, u16* __restrict__ xn, float* __restrict__ st2, float* __restrict__ w1s, unsigned* __restrict__ cnt) {
    const int lane = threadIdx.x & 63;
    if (blockIdx.x == 0 && threadIdx.x < 160) cnt[threadIdx.x * 32] = 0u;
    if (blockIdx.x >= SEQ / 4) {
        __shared__ float red[32][65];
        const int cg = threadIdx.x & 7, rg = threadIdx.x >> 3, col0 = (blockIdx.x - SEQ / 4) * 64;
        const u32x4* src = (const u32x4*)(w1 + (size_t)(rg * 20) * FF + col0 + cg * 8);
        u32x4 t[20];
#pragma unroll
        for (int i = 0; i < 20; ++i) t[i] = src[(size_t)i * (FF / 8)];
        float a[8] = {0.f, 0.f, 0.f, 0.f, 0.f, 0.f, 0.f, 0.f};
#pragma unroll
        for (int i = 0; i < 20; ++i)
#pragma unroll
            for (int e = 0; e < 4; ++e) { a[2 * e] += __uint_as_float(t[i][e] << 16); a[2 * e + 1] += __uint_as_float(t[i][e] & 0xffff0000u); }
#pragma unroll
        for (int e = 0; e < 8; ++e) red[rg][cg * 8 + e] = a[e];
        __syncthreads();
        if (threadIdx.x < 64) { float s = 0.f;
#pragma unroll
            for (int r = 0; r < 32; ++r) s += red[r][threadIdx.x];
            w1s[col0 + threadIdx.x] = s; }
        return;
    }
    const int row = blockIdx.x * 4 + (threadIdx.x >> 6);
    const u32x4* xr = (const u32x4*)(x + (size_t)row * DM);
    const bool two = lane < 16;
    u32x4 w0 = xr[lane], w1v = two ? xr[64 + lane] : (u32x4){0u, 0u, 0u, 0u};
    float v[16];
#pragma unroll
    for (int i = 0; i < 4; ++i) { v[2 * i] = __uint_as_float(w0[i] << 16); v[2 * i + 1] = __uint_as_float(w0[i] & 0xffff0000u);
                                  v[8 + 2 * i] = __uint_as_float(w1v[i] << 16); v[8 + 2 * i + 1] = __uint_as_float(w1v[i] & 0xffff0000u); }
    const size_t base = (size_t)(row >> 5) * 40 * 64 * 8 + (size_t)(row & 31) * 8;
    const size_t off0 = base + (size_t)(lane >> 1) * 512 + (lane & 1) * 256;
    const size_t off1 = base + (size_t)((64 + lane) >> 1) * 512 + (lane & 1) * 256;
#pragma unroll
    for (int pass = 0; pass < 2; ++pass) {
        float s = 0.f;
#pragma unroll
        for (int i = 0; i < 16; ++i) s += v[i];
        const float mean = wave_sum(s) * (1.0f / DM);
        float q = 0.f;
#pragma unroll
        for (int i = 0; i < 8; ++i) { float d = v[i] - mean; q += d * d; }
        if (two) {
#pragma unroll
            for (int i = 8; i < 16; ++i) { float d = v[i] - mean; q += d * d; }
        }
        const float var = wave_sum(q) * (1.0f / DM);
        const float mb = rbf(mean), vb = rbf(var);
        const float den = rbf(__fsqrt_rn(rbf(vb + 1e-5f)));
        if (pass == 1) { if (lane == 0) { st2[2 * row] = mb; st2[2 * row + 1] = den; } break; }
#pragma unroll
        for (int i = 0; i < 16; ++i) v[i] = rbf(__fdiv_rn(rbf(v[i] - mb), den));
        if (!two) {
#pragma unroll
            for (int i = 8; i < 16; ++i) v[i] = 0.f;
        }
        u32x4 a, b;
#pragma unroll
        for (int i = 0; i < 4; ++i) { a[i] = (__float_as_uint(v[2 * i]) >> 16) | (__float_as_uint(v[2 * i + 1]) & 0xffff0000u);
                                      b[i] = (__float_as_uint(v[8 + 2 * i]) >> 16) | (__float_as_uint(v[8 + 2 * i + 1]) & 0xffff0000u); }
        *(u32x4*)(xn + off0) = a;
        if (two) *(u32x4*)(xn + off1) = b;
    }
}

constexpr int SLOT = 24576, LDS_K = 0, LDS_V = 3 * SLOT, PSLOT = 32768, K1_LDS = 163840;
constexpr float CSC = 0.07216878364870322f * 1.4426950408889634f;
constexpr float THR = 8.f;

DI float gelu_tanh(float x) {
    const float e = __builtin_amdgcn_exp2f(x * __builtin_fmaf(x * x, 0.10294324f, 2.3022082f));
    return __builtin_fmaf(-x, __builtin_amdgcn_rcpf(e + 1.f), x);
}
template <int OFF> DI void gld16(bf16x8& d, unsigned voff, const void* sb) { asm volatile("global_load_dwordx4 %0, %1, %2 offset:%3" : "=v"(d) : "v"(voff), "s"(sb), "i"(OFF) : "memory"); }
template <int OFF> DI void gld16f(f32x4& d, unsigned voff, const void* sb) { asm volatile("global_load_dwordx4 %0, %1, %2 offset:%3" : "=v"(d) : "v"(voff), "s"(sb), "i"(OFF) : "memory"); }
template <int OFF> DI void gld8(u32x2& d, unsigned voff, const void* sb) { asm volatile("global_load_dwordx2 %0, %1, %2 offset:%3" : "=v"(d) : "v"(voff), "s"(sb), "i"(OFF) : "memory"); }

template <int I> DI bf16x8 pfrag(lds_cptr wqp, lds_cptr w1p) {
    constexpr int s = I >> 3, j = I & 7;
    if constexpr (j < 6) { constexpr int off = 2 * s * 3072 + (j >> 1) * 1024 + (j & 1) * 512; return cat(vtr(wqp + off), vtr(wqp + off + 256)); }
    else                 { constexpr int off = 2 * s * 1024 + (j - 6) * 512; return cat(vtr(w1p + off), vtr(w1p + off + 256)); }
}
template <int I, bool NEXT, class H> DI void pstep(f32x16 (&o6)[6], f32x16 (&hacc)[2], bf16x8 (&f)[4], const bf16x8 (&xf)[4], lds_cptr wqp, lds_cptr w1p, lds_cptr wqn, lds_cptr w1n, H&& hook) {
    constexpr int s = I >> 3, j = I & 7;
    if constexpr (I + 3 < 32) f[(I + 3) & 3] = pfrag<I + 3>(wqp, w1p);
    else if constexpr (NEXT) f[(I + 3) & 3] = pfrag<I + 3 - 32>(wqn, w1n);
    if constexpr (j < 6) { o6[j] = MFMA(f[I & 3], xf[s], o6[j]); PIN(o6[j]); } else { hacc[j - 6] = MFMA(f[I & 3], xf[s], hacc[j - 6]); PIN(hacc[j - 6]); }
    hook(std::integral_constant<int, I>{});
    asm volatile("" ::: "memory"); SBAR();
    if constexpr (I + 1 < 32) pstep<I + 1, NEXT>(o6, hacc, f, xf, wqp, w1p, wqn, w1n, hook);
}

__global__ __launch_bounds__(512, 2)
void k1_attn(const u16* __restrict__ xn, const float* __restrict__ st2, const float* __restrict__ w1sum, const u16* __restrict__ Kc, const u16* __restrict__ Vc,
             const u16* __restrict__ wq, const u16* __restrict__ w1, const u16* __restrict__ b1,
             u16* __restrict__ o, u16* __restrict__ hbuf, const u16* __restrict__ wo, const u16* __restrict__ w2, u16* __restrict__ wpk
             ) {
    extern __shared__ __attribute__((aligned(16))) char lds[];
    const int tid = threadIdx.x, lane = tid & 63, r32 = lane & 31, hi = lane >> 5;
#define STAMP(i)
    STAMP(0);
    const int wid = __builtin_amdgcn_readfirstlane(tid >> 6);
    const int bid = blockIdx.x, xcd = bid & 7, jj = bid >> 3;
    const int head = xcd + 8 * (jj >> 3), qb = jj & 7;
    const int row0 = qb * 256 + wid * 32;
    const unsigned lds0 = (unsigned)(uintptr_t)lds;
    const lds_cptr L = (lds_cptr)lds;

    const int vd1 = lane >> 5, vr7 = (lane >> 2) & 7, vch = lane & 3;
    const int kr7 = ((lane >> 4) << 1) | (((lane >> 3) & 1) ^ ((wid >> 1) & 1)), kc8 = (lane & 7) ^ kr7;
    const int q4 = (lane & 15) >> 2, p4 = lane & 3, g1 = (lane >> 4) & 1;
    const int r7 = r32 & 7;

    const char* kbase = (const char*)(Kc + (size_t)head * SEQ * DH);
    const char* vbase = (const char*)(Vc + (size_t)head * SEQ * DH);
    const unsigned kvo = kr7 * (DH * 2) + kc8 * 16, vvo = vr7 * (DH * 2) + vd1 * 64 + vch * 16;
    const unsigned kdst = lds0 + LDS_K + wid * 3072, vdst = lds0 + LDS_V + wid * 3072;
#define DMA_K(t, slot) do { const char* s_ = kbase + (size_t)((64 * (t) + 32 + 8 * wid) & (SEQ - 1)) * (DH * 2); \
        gldss(kvo, s_, kdst + (slot)); gldss(kvo, s_ + 128, kdst + (slot) + 1024); gldss(kvo, s_ + 256, kdst + (slot) + 2048); } while (0)
#define DMA_V(t, slot) do { const char* s_ = vbase + (size_t)((64 * (t) + 8 * wid) & (SEQ - 1)) * (DH * 2); \
        gldss(vvo, s_, vdst + (slot)); gldss(vvo, s_ + 128, vdst + (slot) + 1024); gldss(vvo, s_ + 256, vdst + (slot) + 2048); } while (0)

    f32x16 o6[6];
    f32x16 hacc[2];
#pragma unroll
    for (int i = 0; i < 6; ++i) o6[i] = f32x16{};
    hacc[0] = f32x16{}; hacc[1] = f32x16{};
    const int fcol0 = (48 * head < FF - 64) ? 48 * head : FF - 64;
    u32x2 bb[8], stv; f32x4 wsv[8];
    {
        const char* wqs = (const char*)(wq + (size_t)(8 * wid) * DQ + head * DH);
        const char* w1s = (const char*)(w1 + (size_t)(8 * wid) * FF + fcol0);
        const unsigned wqo = vr7 * (DQ * 2) + vd1 * 64 + vch * 16, w1o = vr7 * (FF * 2) + vd1 * 64 + vch * 16;
        const unsigned wqd = lds0 + wid * 3072, w1d = lds0 + 24576 + wid * 1024;
#define CSRC(c) (c)
#define DMA_W(c) do { \
            gldss(wqo, wqs + (size_t)(c) * 64 * DQ * 2,       wqd + ((c) % 5) * PSLOT); \
            gldss(wqo, wqs + (size_t)(c) * 64 * DQ * 2 + 128, wqd + ((c) % 5) * PSLOT + 1024); \
            gldss(wqo, wqs + (size_t)(c) * 64 * DQ * 2 + 256, wqd + ((c) % 5) * PSLOT + 2048); \
            gldss(w1o, w1s + (size_t)(c) * 64 * FF * 2,       w1d + ((c) % 5) * PSLOT); } while (0)
        const int p4s = ((p4 & 1) << 1) | (p4 >> 1);
        const lds_cptr wqp = L + hi * 3072 + q4 * 64 + g1 * 32 + p4s * 8;
        const lds_cptr w1p = L + 24576 + hi * 1024 + q4 * 64 + g1 * 32 + p4 * 8;
        const char* xsrc = (const char*)(xn + (size_t)(row0 >> 5) * 40 * 512);
        const char* bsrc = (const char*)(b1 + fcol0);
        const char* wssrc = (const char*)(w1sum + fcol0);
        const char* stsrc = (const char*)(st2 + 2 * row0);
        const unsigned xo = lane * 16, bo = 8 * hi, wso = 16 * hi, sto = 8 * r32;
        bf16x8 xa[4], xb[4], f[4];
#define LDX(c, X) do { const char* xp_ = xsrc + CSRC(c) * 4096; \
            gld16<0>(X[0], xo, xp_); gld16<1024>(X[1], xo, xp_); gld16<2048>(X[2], xo, xp_); gld16<3072>(X[3], xo, xp_); } while (0)
#define WAITX(N, X) asm volatile("s_waitcnt vmcnt(" #N ") lgkmcnt(0)\n\ts_barrier" : "+v"(X[0]), "+v"(X[1]), "+v"(X[2]), "+v"(X[3]) :: "memory")
#define PCOMP(c, X, PRE, NEXT, HOOK) do { lds_cptr a_ = wqp + ((c) % 5) * PSLOT; lds_cptr b_ = w1p + ((c) % 5) * PSLOT; lds_cptr an_ = wqp + (((c) + 1) % 5) * PSLOT; lds_cptr bn_ = w1p + (((c) + 1) % 5) * PSLOT; \
            PIN(a_); PIN(b_); PIN(an_); PIN(bn_); \
            if (!(PRE)) { f[0] = pfrag<0>(a_, b_); f[1] = pfrag<1>(a_, b_); f[2] = pfrag<2>(a_, b_); } SBAR(); \
            pstep<0, NEXT>(o6, hacc, f, X, a_, b_, an_, bn_, HOOK); } while (0)
#define XHOOK(cn, XN) if constexpr (I == 1) gld16<0>(XN[0], xo, xsrc + (cn) * 4096); if constexpr (I == 4) gld16<1024>(XN[1], xo, xsrc + (cn) * 4096); \
                      if constexpr (I == 7) gld16<2048>(XN[2], xo, xsrc + (cn) * 4096); if constexpr (I == 10) gld16<3072>(XN[3], xo, xsrc + (cn) * 4096);
#define WHOOK(cw) if constexpr (I == 13) gldss(wqo, wqs + (size_t)(cw) * 64 * DQ * 2,       wqd + ((cw) % 5) * PSLOT); \
                  if constexpr (I == 17) gldss(wqo, wqs + (size_t)(cw) * 64 * DQ * 2 + 128, wqd + ((cw) % 5) * PSLOT + 1024); \
                  if constexpr (I == 21) gldss(wqo, wqs + (size_t)(cw) * 64 * DQ * 2 + 256, wqd + ((cw) % 5) * PSLOT + 2048); \
                  if constexpr (I == 25) gldss(w1o, w1s + (size_t)(cw) * 64 * FF * 2,       w1d + ((cw) % 5) * PSLOT);
#define KHOOK(t, slot, I0) { const char* s_ = kbase + (size_t)((64 * (t) + 32 + 8 * wid) & (SEQ - 1)) * (DH * 2); \
                  if constexpr (I == (I0)) gldss(kvo, s_, kdst + (slot)); if constexpr (I == (I0) + 3) gldss(kvo, s_ + 128, kdst + (slot) + 1024); if constexpr (I == (I0) + 6) gldss(kvo, s_ + 256, kdst + (slot) + 2048); }
#define VHOOK(t, slot, I0) { const char* s_ = vbase + (size_t)((64 * (t) + 8 * wid) & (SEQ - 1)) * (DH * 2); \
                  if constexpr (I == (I0)) gldss(vvo, s_, vdst + (slot)); if constexpr (I == (I0) + 3) gldss(vvo, s_ + 128, vdst + (slot) + 1024); if constexpr (I == (I0) + 6) gldss(vvo, s_ + 256, vdst + (slot) + 2048); }
#define HK(body) [&](auto Ic) { constexpr int I = decltype(Ic)::value; body }
        LDX(0, xa); DMA_W(0); DMA_W(1); DMA_W(2); DMA_W(3);
        WAITX(12, xa); PCOMP(0, xa, false, false, HK(XHOOK(1, xb) WHOOK(4)));
        WAITX(4, xb);  PCOMP(1, xb, false, true,  HK(XHOOK(2, xa) WHOOK(5)));
        WAITX(4, xa);  PCOMP(2, xa, true,  true,  HK(XHOOK(3, xb) WHOOK(6)));
        WAITX(4, xb);  PCOMP(3, xb, true,  true,  HK(XHOOK(4, xa) WHOOK(7)));
        WAITX(4, xa);  PCOMP(4, xa, true,  true,  HK(XHOOK(5, xb) WHOOK(8)));
        WAITX(4, xb);  PCOMP(5, xb, true,  true,  HK(XHOOK(6, xa) WHOOK(9)));
        WAITX(4, xa);  PCOMP(6, xa, true,  true,  HK(XHOOK(7, xb) KHOOK(-1, 0, 13)));
        WAITX(3, xb);  PCOMP(7, xb, true,  true,  HK(XHOOK(8, xa) KHOOK(0, SLOT, 13)));
        WAITX(3, xa);  PCOMP(8, xa, true,  true,  HK(XHOOK(9, xb) KHOOK(1, 2 * SLOT, 13) VHOOK(0, 0, 22)));
        WAITX(6, xb);  PCOMP(9, xb, true,  false, HK(
            if constexpr (I == 1) { gld8<0>(bb[0], bo, bsrc); gld8<16>(bb[1], bo, bsrc); } if constexpr (I == 2) { gld8<32>(bb[2], bo, bsrc); gld8<48>(bb[3], bo, bsrc); }
            if constexpr (I == 3) { gld8<64>(bb[4], bo, bsrc); gld8<80>(bb[5], bo, bsrc); } if constexpr (I == 4) { gld8<96>(bb[6], bo, bsrc); gld8<112>(bb[7], bo, bsrc); }
            if constexpr (I == 5) { gld16f<0>(wsv[0], wso, wssrc); gld16f<32>(wsv[1], wso, wssrc); } if constexpr (I == 6) { gld16f<64>(wsv[2], wso, wssrc); gld16f<96>(wsv[3], wso, wssrc); }
            if constexpr (I == 7) { gld16f<128>(wsv[4], wso, wssrc); gld16f<160>(wsv[5], wso, wssrc); } if constexpr (I == 8) { gld16f<192>(wsv[6], wso, wssrc); gld16f<224>(wsv[7], wso, wssrc); }
            if constexpr (I == 9) gld8<0>(stv, sto, stsrc);
            VHOOK(1, SLOT, 13)));
        asm volatile("s_waitcnt vmcnt(3)" : "+v"(bb[0]), "+v"(bb[1]), "+v"(bb[2]), "+v"(bb[3]), "+v"(bb[4]), "+v"(bb[5]), "+v"(bb[6]), "+v"(bb[7]),
                     "+v"(wsv[0]), "+v"(wsv[1]), "+v"(wsv[2]), "+v"(wsv[3]), "+v"(wsv[4]), "+v"(wsv[5]), "+v"(wsv[6]), "+v"(wsv[7]), "+v"(stv) :: "memory");
#undef DMA_W
#undef LDX
#undef WAITX
#undef PCOMP
    }
    STAMP(1);
    bf16x8 qf[12];
#pragma unroll
    for (int db = 0; db < 6; ++db)
#pragma unroll
        for (int s = 0; s < 2; ++s) {
            u32x4 w;
#pragma unroll
            for (int e = 0; e < 4; ++e) w[e] = cvtpk(o6[db][8 * s + 2 * e] * CSC, o6[db][8 * s + 2 * e + 1] * CSC);
            qf[2 * db + s] = __builtin_bit_cast(bf16x8, w);
        }
    {
        const int flo = 48 * head - fcol0;
        const float m2 = __uint_as_float(stv[0]), rden = __fdiv_rn(1.0f, __uint_as_float(stv[1]));
        char* hbase = (char*)hbuf + (size_t)(row0 >> 5) * 96 * 1024 + r32 * 16 + 8 * hi;
#pragma unroll
        for (int ft = 0; ft < 2; ++ft)
#pragma unroll
            for (int g = 0; g < 4; ++g) {
                const int fl = 32 * ft + 8 * g;
                if (fl >= flo && fl < flo + 48) {
                    float hv[4];
#pragma unroll
                    for (int e = 0; e < 4; ++e) {
                        const unsigned bw = bb[4 * ft + g][e >> 1];
                        const float bias = (e & 1) ? __uint_as_float(bw & 0xffff0000u) : __uint_as_float(bw << 16);
                        hv[e] = gelu_tanh(rbf(rbf(__builtin_fmaf(-m2, wsv[4 * ft + g][e], hacc[ft][4 * g + e]) * rden) + bias));
                    }
                    u32x2 w; w[0] = cvtpk(hv[0], hv[1]); w[1] = cvtpk(hv[2], hv[3]);
                    const int fc = (fcol0 + fl) >> 3;
                    *(u32x2*)(hbase + (fc >> 1) * 1024 + (fc & 1) * 512) = w;
                }
            }
    }
#pragma unroll
    for (int i = 0; i < 6; ++i) o6[i] = f32x16{};

    lds_cptr kp[4];
#pragma unroll
    for (int j = 0; j < 4; ++j)
        kp[j] = L + LDS_K + (r32 >> 3) * 3072 + ((r7 >> 1) << 8) + ((((r32 >> 4) & 1) ^ (r7 & 1)) << 7) + ((r7 ^ (2 * j + hi)) << 4);
    const lds_cptr vp = L + LDS_V + (4 * hi + q4) * 64 + g1 * 32 + p4 * 8;

    f32x16 SA, SB, NEGM; float mref, lsum = 0.f;
    u32x4 P0 = {0u,0u,0u,0u}, P1 = {0u,0u,0u,0u};
#define KRD(sl_, half, ks) ldsr(kp[(ks) & 3] + (sl_) + (half) * 12288 + ((ks) >> 2) * 1024)
#define VRD(sl_, half, s, db) cat(vtr(vp + (sl_) + (4 * (half) + 2 * (s)) * 3072 + ((db) >> 1) * 1024 + ((db) & 1) * 512), \
                                  vtr(vp + (sl_) + (4 * (half) + 2 * (s) + 1) * 3072 + ((db) >> 1) * 1024 + ((db) & 1) * 512))
    {
        SA = f32x16{};
#pragma unroll
        for (int ks = 0; ks < 12; ++ks) SA = MFMA(KRD(0, 1, ks), qf[ks], SA);
        float mx = SA[0];
#pragma unroll
        for (int i = 1; i < 16; ++i) mx = __builtin_fmaxf(mx, SA[i]);
        mref = xhalf_max(mx);
#pragma unroll
        for (int i = 0; i < 16; ++i) { SA[i] -= mref; NEGM[i] = -mref; }
    }
    WAIT_BAR(3);

#define EX(v) __builtin_amdgcn_exp2f(v)
#define MX3(a, b, c) max3a((a), (b), (c))
#define GEND() do { asm volatile("" ::: "memory"); SBAR(); } while (0)
#define A_CVT(x) x
#define A_ADD(x) x
#define A_MAX(x) x
#define A_BR 1
#define HSTEP(SC, SN, ksl, khalf, vsl, vhalf, nksl, nkhalf, D0, D1, D2, D3, D4, D5) do { \
        float sa = 0.f, sb = 0.f, mx = 0.f; \
        k3 = KRD(ksl, khalf, 3);  SN = MFMA(k0, qf[0], NEGM); PIN(SN); float p0 = EX(SC[0]); PIN(p0); GEND(); \
        k0 = KRD(ksl, khalf, 4);  SN = MFMA(k1, qf[1], SN); PIN(SN); D0; float p1 = EX(SC[1]); PIN(p1); GEND(); \
        k1 = KRD(ksl, khalf, 5);  SN = MFMA(k2, qf[2], SN); PIN(SN); float p2 = EX(SC[2]); A_CVT(P0[0] = cvtpk(p0, p1);) PIN(p2); PIN(P0); GEND(); \
        k2 = KRD(ksl, khalf, 6);  SN = MFMA(k3, qf[3], SN); PIN(SN); float p3 = EX(SC[3]); A_ADD(sa = p0 + p1;) PIN(p3); PIN(sa); GEND(); \
        k3 = KRD(ksl, khalf, 7);  SN = MFMA(k0, qf[4], SN); PIN(SN); float p4 = EX(SC[4]); A_CVT(P0[1] = cvtpk(p2, p3);) PIN(p4); PIN(P0); GEND(); \
        k0 = KRD(ksl, khalf, 8);  SN = MFMA(k1, qf[5], SN); PIN(SN); D1; float p5 = EX(SC[5]); A_ADD(sb = p2 + p3;) PIN(p5); PIN(sb); GEND(); \
        k1 = KRD(ksl, khalf, 9);  SN = MFMA(k2, qf[6], SN); PIN(SN); float p6 = EX(SC[6]); A_CVT(P0[2] = cvtpk(p4, p5);) PIN(p6); PIN(P0); GEND(); \
        k2 = KRD(ksl, khalf, 10); SN = MFMA(k3, qf[7], SN); PIN(SN); float p7 = EX(SC[7]); A_ADD(sa += p4;) A_ADD(sb += p5;) PIN(p7); PIN(sa); PIN(sb); GEND(); \
        k3 = KRD(ksl, khalf, 11); SN = MFMA(k0, qf[8], SN); PIN(SN); float p8 = EX(SC[8]); A_CVT(P0[3] = cvtpk(p6, p7);) PIN(p8); PIN(P0); GEND(); \
        bf16x8 v0 = VRD(vsl, vhalf, 0, 0); SN = MFMA(k1, qf[9], SN); PIN(SN); D2; float p9 = EX(SC[9]); A_ADD(sa += p6;) A_ADD(sb += p7;) PIN(p9); PIN(sa); PIN(sb); GEND(); \
        bf16x8 v1 = VRD(vsl, vhalf, 0, 1); SN = MFMA(k2, qf[10], SN); PIN(SN); float p10 = EX(SC[10]); A_CVT(P1[0] = cvtpk(p8, p9);) PIN(p10); PIN(P1); GEND(); \
        bf16x8 v2 = VRD(vsl, vhalf, 0, 2); SN = MFMA(k3, qf[11], SN); PIN(SN); float p11 = EX(SC[11]); A_ADD(sa += p8;) A_ADD(sb += p9;) PIN(p11); PIN(sa); PIN(sb); GEND(); \
        const bf16x8 pa0 = __builtin_bit_cast(bf16x8, P0); \
        bf16x8 v3; \
        v3 = VRD(vsl, vhalf, 0, 3); o6[0] = MFMA(v0, pa0, o6[0]); PIN(o6[0]); float p12 = EX(SC[12]); A_CVT(P1[1] = cvtpk(p10, p11);) PIN(p12); PIN(P1); GEND(); \
        v0 = VRD(vsl, vhalf, 0, 4); o6[1] = MFMA(v1, pa0, o6[1]); PIN(o6[1]); D3; float p13 = EX(SC[13]); A_ADD(sa += p10;) A_ADD(sb += p11;) PIN(p13); PIN(sa); PIN(sb); GEND(); \
        v1 = VRD(vsl, vhalf, 0, 5); o6[2] = MFMA(v2, pa0, o6[2]); PIN(o6[2]); float p14 = EX(SC[14]); A_CVT(P1[2] = cvtpk(p12, p13);) PIN(p14); PIN(P1); GEND(); \
        v2 = VRD(vsl, vhalf, 1, 0); o6[3] = MFMA(v3, pa0, o6[3]); PIN(o6[3]); float p15 = EX(SC[15]); A_ADD(sa += p12;) A_ADD(sb += p13;) PIN(p15); PIN(sa); PIN(sb); GEND(); \
        v3 = VRD(vsl, vhalf, 1, 1); o6[4] = MFMA(v0, pa0, o6[4]); PIN(o6[4]); A_CVT(P1[3] = cvtpk(p14, p15);) A_MAX(mx = MX3(SN[0], SN[1], SN[2]);) A_MAX(mx = MX3(mx, SN[3], SN[4]);) PIN(P1); PIN(mx); GEND(); \
        v0 = VRD(vsl, vhalf, 1, 2); o6[5] = MFMA(v1, pa0, o6[5]); PIN(o6[5]); D4; A_ADD(sa += p14;) A_ADD(sb += p15;) A_MAX(mx = MX3(mx, SN[5], SN[6]);) PIN(sa); PIN(sb); PIN(mx); GEND(); \
        const bf16x8 pa1 = __builtin_bit_cast(bf16x8, P1); \
        v1 = VRD(vsl, vhalf, 1, 3); o6[0] = MFMA(v2, pa1, o6[0]); PIN(o6[0]); A_MAX(mx = MX3(mx, SN[7], SN[8]);) lsum += sa + sb; PIN(mx); PIN(lsum); GEND(); \
        v2 = VRD(vsl, vhalf, 1, 4); o6[1] = MFMA(v3, pa1, o6[1]); PIN(o6[1]); A_MAX(mx = MX3(mx, SN[9], SN[10]);) A_MAX(mx = MX3(mx, SN[11], SN[12]);) PIN(mx); GEND(); \
        v3 = VRD(vsl, vhalf, 1, 5); o6[2] = MFMA(v0, pa1, o6[2]); PIN(o6[2]); A_MAX(mx = MX3(mx, SN[13], SN[14]);) A_MAX(mx = max3a(mx, SN[15], SN[15]);) PIN(mx); GEND(); \
        k0 = KRD(nksl, nkhalf, 0);  o6[3] = MFMA(v1, pa1, o6[3]); PIN(o6[3]); D5; GEND(); \
        k1 = KRD(nksl, nkhalf, 1);  o6[4] = MFMA(v2, pa1, o6[4]); PIN(o6[4]); GEND(); \
        k2 = KRD(nksl, nkhalf, 2);  o6[5] = MFMA(v3, pa1, o6[5]); PIN(o6[5]); GEND(); \
        if (A_BR && __builtin_expect(__any(mx > THR), 0)) { \
            const float dl = __builtin_fmaxf(xhalf_max(mx), 0.f); mref += dl; const float f = __builtin_amdgcn_exp2f(-dl); lsum *= f; \
            _Pragma("unroll") for (int r_ = 0; r_ < 16; ++r_) { SN[r_] -= dl; NEGM[r_] = -mref; } \
            _Pragma("unroll") for (int d_ = 0; d_ < 6; ++d_) _Pragma("unroll") for (int r_ = 0; r_ < 16; ++r_) o6[d_][r_] *= f; \
        } \
    } while (0)

    bf16x8 k0 = KRD(SLOT, 0, 0), k1 = KRD(SLOT, 0, 1), k2 = KRD(SLOT, 0, 2), k3;
    u32x4 pkv = {0u, 0u, 0u, 0u};
    const int pk0 = (bid * 8 + wid) * 5;
    const unsigned pko = vr7 * (DM * 2) + vd1 * 64 + vch * 16, xo16 = lane * 16;
    STAMP(2);
    int s0 = 0;
    for (int t = 0; t < 32; ++t) {
        const int s1 = (s0 == 2 * SLOT) ? 0 : s0 + SLOT;
        const int s2 = (s0 == 0) ? 2 * SLOT : s0 - SLOT;
        if (t >= 1 && t <= 5 && pk0 + t - 1 < 9600) asm volatile("global_store_dwordx4 %0, %1, %2\n\ts_nop 1" :: "v"(xo16), "v"(pkv), "s"((const char*)wpk + (size_t)(pk0 + t - 1) * 1024) : "memory");
        if (t < 5 && pk0 + t < 9600) {
            const int P = pk0 + t, b = P >> 4, tn = b / 120, g = b - tn * 120;
            const char* src = ((g < 96) ? (const char*)(wo + (size_t)g * 64 * DM) : (const char*)(w2 + (size_t)(g - 96) * 64 * DM)) + (size_t)(8 * ((P >> 1) & 7)) * (DM * 2) + tn * 256 + (P & 1) * 128;
            asm volatile("global_load_dwordx4 %0, %1, %2" : "+v"(pkv) : "v"(pko), "s"(src) : "memory");
        }
        const char* ks_ = kbase + (size_t)((64 * (t + 2) + 32 + 8 * wid) & (SEQ - 1)) * (DH * 2);
        const char* vs_ = vbase + (size_t)((64 * (t + 2) + 8 * wid) & (SEQ - 1)) * (DH * 2);
        HSTEP(SA, SB, s1, 0, s0, 0, s1, 1, gldss(kvo, ks_, kdst + s0), gldss(kvo, ks_ + 128, kdst + s0 + 1024), gldss(kvo, ks_ + 256, kdst + s0 + 2048),
              gldss(vvo, vs_, vdst + s2), gldss(vvo, vs_ + 128, vdst + s2 + 1024), gldss(vvo, vs_ + 256, vdst + s2 + 2048));
        HSTEP(SB, SA, s1, 1, s0, 1, s2, 0, (void)0, (void)0, (void)0, (void)0, (void)0, (void)0);
        WAIT_BAR(3);
        s0 = s1;
    }
    STAMP(3);
    asm volatile("s_waitcnt vmcnt(0)" ::: "memory");
    {
        const float il = __builtin_amdgcn_rcpf(xhalf_sum(lsum));
        char* obase = (char*)o + ((size_t)(row0 >> 5) * 384 + head * 12) * 1024 + r32 * 16 + 8 * hi;
#pragma unroll
        for (int db = 0; db < 6; ++db)
#pragma unroll
            for (int g = 0; g < 4; ++g) {
                u32x2 w; w[0] = cvtpk(o6[db][4 * g] * il, o6[db][4 * g + 1] * il); w[1] = cvtpk(o6[db][4 * g + 2] * il, o6[db][4 * g + 3] * il);
                *(u32x2*)(obase + (2 * db + (g >> 1)) * 1024 + (g & 1) * 512) = w;
            }
    }
    STAMP(4);
}

constexpr int K2_STAGE = 16384, K2_LDS = 4 * K2_STAGE + 64;
DI void st16_sc1(const void* sb, unsigned voff, f32x4 v) { asm volatile("global_store_dwordx4 %0, %1, %2 sc1\n\ts_nop 1" :: "v"(voff), "v"(v), "s"(sb) : "memory"); }
template <int OFF> DI void ld16_sc1(f32x4& d, unsigned voff, const void* sb) { asm volatile("global_load_dwordx4 %0, %1, %2 offset:%3 sc1" : "=v"(d) : "v"(voff), "s"(sb), "i"(OFF) : "memory"); }
__global__ __launch_bounds__(512, 2)
void k2_gemm(const u16* __restrict__ o, const u16* __restrict__ hbuf, const u16* __restrict__ wpk, float* __restrict__ part, unsigned* __restrict__ cnt,
             const u16* __restrict__ x, const u16* __restrict__ b2, u16* __restrict__ out
             ) {
    extern __shared__ __attribute__((aligned(16))) char lds[];
    const int tid = threadIdx.x, lane = tid & 63, r32 = lane & 31, hi = lane >> 5;
#define STAMP2(i)
    STAMP2(0);
    const int wid = __builtin_amdgcn_readfirstlane(tid >> 6), wm = wid & 3, kh = wid >> 2;
    const int bid = blockIdx.x;
    const int lin = (bid & 7) * 30 + (bid >> 3);
    const int chunk = lin / 80, tile = lin - chunk * 80, tm = tile / 5, tn = tile - tm * 5;
    const unsigned lds0 = (unsigned)(uintptr_t)lds;
    const lds_cptr L = (lds_cptr)lds;
    const char* Ao = (const char*)o + ((size_t)(4 * tm + wm) * 384 + 2 * kh) * 1024;
    const char* Ah = (const char*)hbuf + ((size_t)(4 * tm + wm) * 96 + 2 * kh) * 1024;
    const char* Wp = (const char*)wpk + (size_t)tn * 120 * 16384 + wid * 2048;
    const unsigned avo = lane * 16;
    const unsigned wdst = lds0 + wid * 2048;
    const int g0 = chunk * 40;
    const int q4 = (lane & 15) >> 2, p4 = lane & 3, g1 = (lane >> 4) & 1;
    const lds_cptr wp = L + (4 * kh + hi) * 2048 + q4 * 64 + g1 * 32 + p4 * 8;
    f32x16 acc[4], sv[4];
#pragma unroll
    for (int i = 0; i < 4; ++i) acc[i] = f32x16{};
    bf16x8 ab[4][2];
#define SRC2(st) const int g_ = g0 + (((st) < 40) ? (st) : 39); const char* w_ = Wp + (size_t)g_ * 16384; \
        const char* a_ = (g_ < 96) ? Ao + (size_t)g_ * 4096 : Ah + (size_t)(g_ - 96) * 4096;
#define ISSUE2(st, sl) do { SRC2(st); gld16<0>(ab[sl][0], avo, a_); gld16<1024>(ab[sl][1], avo, a_); \
        gldss(avo, w_, wdst + (sl) * K2_STAGE); gldss(avo, w_ + 1024, wdst + (sl) * K2_STAGE + 1024); } while (0)
    ISSUE2(0, 0); ISSUE2(1, 1); ISSUE2(2, 2);
#define WF(sl, s, ni) cat(vtr(wp + (sl) * K2_STAGE + (s) * 4096 + ((ni) >> 1) * 1024 + ((ni) & 1) * 512), vtr(wp + (sl) * K2_STAGE + (s) * 4096 + ((ni) >> 1) * 1024 + ((ni) & 1) * 512 + 256))
#define NS(sl) (((sl) + 3) & 3)
#define STAGE2(st, sl) do { \
        asm volatile("s_waitcnt vmcnt(8) lgkmcnt(0)\n\ts_barrier" : "+v"(ab[sl][0]), "+v"(ab[sl][1]) :: "memory"); \
        SRC2((st) + 3); \
        bf16x8 f0 = WF(sl, 0, 0), f1 = WF(sl, 0, 1), f2 = WF(sl, 0, 2), f3; SBAR(); \
        f3 = WF(sl, 0, 3); acc[0] = MFMA(f0, ab[sl][0], acc[0]); gld16<0>(ab[NS(sl)][0], avo, a_); SBAR(); \
        f0 = WF(sl, 1, 0); acc[1] = MFMA(f1, ab[sl][0], acc[1]); gld16<1024>(ab[NS(sl)][1], avo, a_); SBAR(); \
        f1 = WF(sl, 1, 1); acc[2] = MFMA(f2, ab[sl][0], acc[2]); gldss(avo, w_, wdst + NS(sl) * K2_STAGE); SBAR(); \
        f2 = WF(sl, 1, 2); acc[3] = MFMA(f3, ab[sl][0], acc[3]); SBAR(); \
        f3 = WF(sl, 1, 3); acc[0] = MFMA(f0, ab[sl][1], acc[0]); gldss(avo, w_ + 1024, wdst + NS(sl) * K2_STAGE + 1024); SBAR(); \
        acc[1] = MFMA(f1, ab[sl][1], acc[1]); SBAR(); \
        acc[2] = MFMA(f2, ab[sl][1], acc[2]); SBAR(); \
        acc[3] = MFMA(f3, ab[sl][1], acc[3]); SBAR(); \
    } while (0)
    STAMP2(1);
    for (int s4 = 0; s4 < 16; s4 += 4) { STAGE2(s4, 0); STAGE2(s4 + 1, 1); STAGE2(s4 + 2, 2); STAGE2(s4 + 3, 3); }
#pragma unroll
    for (int i = 0; i < 4; ++i) { sv[i] = acc[i]; if (chunk == 2) acc[i] = f32x16{}; }
    for (int s4 = 16; s4 < 40; s4 += 4) { STAGE2(s4, 0); STAGE2(s4 + 1, 1); STAGE2(s4 + 2, 2); STAGE2(s4 + 3, 3); }
    STAMP2(2);
    asm volatile("s_waitcnt vmcnt(0) lgkmcnt(0)\n\ts_barrier" : "+v"(ab[0][0]), "+v"(ab[0][1]), "+v"(ab[1][0]), "+v"(ab[1][1]), "+v"(ab[2][0]), "+v"(ab[2][1]) :: "memory");
    char* pbase = (char*)part + (size_t)tile * 4 * 65536 + wid * 8192;
    float* xw = (float*)(lds + wid * 8192); const float* xr = (const float*)(lds + (wid ^ 4) * 8192);
    auto exchange = [&](const f32x16 (&ac)[4], f32x4 (&res)[8]) {
#pragma unroll
        for (int ni = 0; ni < 2; ++ni)
#pragma unroll
            for (int g = 0; g < 4; ++g) { const f32x16& a_ = kh ? ac[ni] : ac[2 + ni]; f32x4 v = {a_[4 * g], a_[4 * g + 1], a_[4 * g + 2], a_[4 * g + 3]}; *(f32x4*)(xw + ((ni * 4 + g) * 64 + lane) * 4) = v; }
        asm volatile("s_waitcnt vmcnt(0) lgkmcnt(0)\n\ts_barrier" ::: "memory");
#pragma unroll
        for (int ni = 0; ni < 2; ++ni)
#pragma unroll
            for (int g = 0; g < 4; ++g) { const f32x16& a_ = kh ? ac[2 + ni] : ac[ni];
                const f32x4 o_ = *(const f32x4*)(xr + ((ni * 4 + g) * 64 + lane) * 4);
                f32x4 v;
                if (kh) { v[0] = o_[0] + a_[4 * g]; v[1] = o_[1] + a_[4 * g + 1]; v[2] = o_[2] + a_[4 * g + 2]; v[3] = o_[3] + a_[4 * g + 3]; }
                else    { v[0] = a_[4 * g] + o_[0]; v[1] = a_[4 * g + 1] + o_[1]; v[2] = a_[4 * g + 2] + o_[2]; v[3] = a_[4 * g + 3] + o_[3]; }
                res[ni * 4 + g] = v; }
        asm volatile("s_waitcnt lgkmcnt(0)\n\ts_barrier" ::: "memory");
    };
    f32x4 ra[8], rb[8];
    if (chunk == 2) { exchange(sv, ra); exchange(acc, rb); } else exchange(acc, ra);
    STAMP2(3);
    const int row = tm * 128 + wm * 32 + r32, col0 = tn * 128 + kh * 64 + 4 * hi;
    auto finish = [&](auto CH) {
        constexpr int C = decltype(CH)::value;
        constexpr int NU = (C == 2) ? 2 : 3;
#pragma unroll
        for (int i = 0; i < 8; ++i) if (i % 3 != C) st16_sc1(pbase + (size_t)C * 65536 + i * 1024, avo, ra[i]);
        if constexpr (C == 2) {
#pragma unroll
            for (int i = 0; i < 8; ++i) if (i % 3 != 2) st16_sc1(pbase + (size_t)3 * 65536 + i * 1024, avo, rb[i]);
        }
        u32x2 xb[NU], bb[NU];
#pragma unroll
        for (int u = 0; u < NU; ++u) { const int i = C + 3 * u; xb[u] = *(const u32x2*)(x + (size_t)row * DM + col0 + 32 * (i >> 2) + 8 * (i & 3)); bb[u] = *(const u32x2*)(b2 + col0 + 32 * (i >> 2) + 8 * (i & 3)); }
        asm volatile("s_waitcnt vmcnt(0)\n\ts_barrier" ::: "memory");
        STAMP2(0);
        if (tid == 0) {
            __hip_atomic_fetch_add(cnt + tile * 64, 1u, __ATOMIC_RELAXED, __HIP_MEMORY_SCOPE_AGENT);
            for (int spin = 0; spin < (1 << 22); ++spin) {
                if (__hip_atomic_load(cnt + tile * 64, __ATOMIC_RELAXED, __HIP_MEMORY_SCOPE_AGENT) >= 3u) break;
                __builtin_amdgcn_s_sleep(1);
            }
        }
        asm volatile("s_waitcnt vmcnt(0) lgkmcnt(0)\n\ts_barrier" ::: "memory");
        STAMP2(1);
        constexpr int SA_ = (C == 0) ? 1 : 0, SB_ = (C == 2) ? 1 : 2;
        f32x4 pa[3], pb[3], pg[3];
#pragma unroll
        for (int u = 0; u < NU; ++u) { const int i = C + 3 * u;
            ld16_sc1<0>(pa[u], avo, pbase + (size_t)SA_ * 65536 + i * 1024); ld16_sc1<0>(pb[u], avo, pbase + (size_t)SB_ * 65536 + i * 1024);
            if constexpr (C != 2) ld16_sc1<0>(pg[u], avo, pbase + (size_t)3 * 65536 + i * 1024); }
        if constexpr (C != 2) asm volatile("s_waitcnt vmcnt(0)" : "+v"(pa[0]), "+v"(pa[1]), "+v"(pa[2]), "+v"(pb[0]), "+v"(pb[1]), "+v"(pb[2]), "+v"(pg[0]), "+v"(pg[1]), "+v"(pg[2]) :: "memory");
        else                  asm volatile("s_waitcnt vmcnt(0)" : "+v"(pa[0]), "+v"(pa[1]), "+v"(pb[0]), "+v"(pb[1]) :: "memory");
        STAMP2(2);
#pragma unroll
        for (int u = 0; u < NU; ++u) { const int i = C + 3 * u;
            float r[4];
#pragma unroll
            for (int j = 0; j < 4; ++j) {
                const float xv = (j & 1) ? __uint_as_float(xb[u][j >> 1] & 0xffff0000u) : __uint_as_float(xb[u][j >> 1] << 16);
                const float bv = (j & 1) ? __uint_as_float(bb[u][j >> 1] & 0xffff0000u) : __uint_as_float(bb[u][j >> 1] << 16);
                const float p0 = (C == 0) ? ra[i][j] : pa[u][j];
                const float p1 = (C == 0) ? pa[u][j] : (C == 1) ? ra[i][j] : pb[u][j];
                const float p2 = (C == 2) ? ra[i][j] : pb[u][j];
                const float gv = (C == 2) ? rb[i][j] : pg[u][j];
                const float s = rbf((p0 + p1) + p2);
                const float mm = rbf(rbf(gv) + bv);
                const float gt = rbf(mm * __builtin_amdgcn_rcpf(1.f + __builtin_amdgcn_exp2f(-mm * 1.4426950408889634f)));
                r[j] = xv + rbf(s * gt);
            }
            u32x2 w; w[0] = cvtpk(r[0], r[1]); w[1] = cvtpk(r[2], r[3]);
            *(u32x2*)(out + (size_t)row * DM + col0 + 32 * (i >> 2) + 8 * (i & 3)) = w;
        }
    };
    if (chunk == 0) finish(std::integral_constant<int, 0>{}); else if (chunk == 1) finish(std::integral_constant<int, 1>{}); else finish(std::integral_constant<int, 2>{});
    STAMP2(4);
}

extern "C" void kernel_launch(void* const* d_in, const int* in_sizes, int n_in, void* d_out, int out_size, void* d_ws, size_t ws_size, hipStream_t stream) {
    const u16* x  = (const u16*)d_in[0];
    const u16* kc = (const u16*)d_in[1];
    const u16* vc = (const u16*)d_in[2];
    const u16* wq = (const u16*)d_in[3];
    const u16* wo = (const u16*)d_in[4];
    const u16* w1 = (const u16*)d_in[6];
    const u16* b1 = (const u16*)d_in[7];
    const u16* w2 = (const u16*)d_in[8];
    const u16* b2 = (const u16*)d_in[9];
    char* ws = (char*)d_ws;
    u16* xn = (u16*)ws;
    float* st2 = (float*)(ws + (size_t)4 * 1024 * 1024);
    float* w1s = (float*)(ws + (size_t)5 * 1024 * 1024);
    u16* hb = (u16*)(ws + (size_t)8 * 1024 * 1024);
    u16* ob = (u16*)(ws + (size_t)16 * 1024 * 1024);
    u16* wpk = (u16*)(ws + (size_t)96 * 1024 * 1024);
    unsigned* cnt = (unsigned*)(ws + (size_t)6 * 1024 * 1024);
    float* part = (float*)(ws + (size_t)48 * 1024 * 1024);
    hipFuncSetAttribute((const void*)k1_attn, hipFuncAttributeMaxDynamicSharedMemorySize, K1_LDS);
    hipFuncSetAttribute((const void*)k2_gemm, hipFuncAttributeMaxDynamicSharedMemorySize, K2_LDS);
    hipLaunchKernelGGL(k0_ln, dim3(K0_LN + K0_WS), dim3(256), 0, stream, x, w1, xn, st2, w1s, cnt);
    hipLaunchKernelGGL(k1_attn, dim3(256), dim3(512), K1_LDS, stream, xn, st2, w1s, kc, vc, wq, w1, b1, ob, hb, wo, w2, wpk
        );
    hipLaunchKernelGGL(k2_gemm, dim3(240), dim3(512), K2_LDS, stream, ob, hb, wpk, part, cnt, x, b2, (u16*)d_out
        );
}
```

```cpp
#include <hip/hip_runtime.h>
#include <hip/hip_bf16.h>
#include <stdint.h>
#include <type_traits>

using bf16x8 = __attribute__((ext_vector_type(8))) short;
using s16x4  = __attribute__((ext_vector_type(4))) short;
using f32x16 = __attribute__((ext_vector_type(16))) float;
using f32x4  = __attribute__((ext_vector_type(4))) float;
using u32x4  = __attribute__((ext_vector_type(4))) unsigned;
using u32x2  = __attribute__((ext_vector_type(2))) unsigned;
typedef short v4i16_t __attribute__((ext_vector_type(4)));
typedef __attribute__((address_space(3))) const char* lds_cptr;
typedef unsigned short u16;

constexpr int SEQ = 2048, DM = 640, NH = 32, DH = 192, DQ = 6144, FF = 1536;

#define DI __device__ __forceinline__
#define SBAR() __builtin_amdgcn_sched_barrier(0)
#define PIN(x) asm volatile("" : "+v"(x))
#define MFMA(a, b, c) __builtin_amdgcn_mfma_f32_32x32x16_bf16(a, b, c, 0, 0, 0)
#define WAIT_BAR(N) asm volatile("s_waitcnt vmcnt(" #N ") lgkmcnt(0)\n\ts_barrier" ::: "memory")

DI float bf2f(u16 b) { return __uint_as_float((unsigned)b << 16); }
typedef float f32x2 __attribute__((ext_vector_type(2)));
typedef __bf16 bf16x2_t __attribute__((ext_vector_type(2)));
DI unsigned cvtpk(float lo, float hi) { f32x2 v = {lo, hi}; bf16x2_t b = __builtin_convertvector(v, bf16x2_t); return __builtin_bit_cast(unsigned, b); }
DI float rbf(float x) { return __uint_as_float(cvtpk(x, x) << 16); }
DI void glds16(const void* g, unsigned lds_base) {
    unsigned sv;
    asm volatile("s_mov_b32 %0, m0\n\ts_mov_b32 m0, %2\n\ts_nop 0\n\tglobal_load_lds_dwordx4 %1, off\n\ts_mov_b32 m0, %0" : "=&s"(sv) : "v"(g), "s"(lds_base) : "memory");
}
DI void gldss(unsigned voff, const void* sbase, unsigned lds_base) {
    asm volatile("s_mov_b32 m0, %2\n\ts_nop 0\n\tglobal_load_lds_dwordx4 %0, %1" :: "v"(voff), "s"(sbase), "s"(lds_base) : "memory");
}
DI bf16x8 ldsr(lds_cptr p) { return *(const __attribute__((address_space(3))) bf16x8*)p; }
DI s16x4 vtr(lds_cptr p) { return __builtin_bit_cast(s16x4, __builtin_amdgcn_ds_read_tr16_b64_v4i16((__attribute__((address_space(3))) v4i16_t*)p)); }
DI bf16x8 cat(s16x4 a, s16x4 b) { return __builtin_shufflevector(a, b, 0, 1, 2, 3, 4, 5, 6, 7); }
DI unsigned rfl(unsigned x) { return (unsigned)__builtin_amdgcn_readfirstlane((int)x); }
DI float max3a(float a, float b, float c) { float r; asm("v_max3_f32 %0, %1, %2, %3" : "=v"(r) : "v"(a), "v"(b), "v"(c)); return r; }
DI float xhalf_max(float m) { auto rr = __builtin_amdgcn_permlane32_swap(__float_as_uint(m), __float_as_uint(m), false, false); return __builtin_fmaxf(__uint_as_float(rr[0]), __uint_as_float(rr[1])); }
DI float xhalf_sum(float m) { auto rr = __builtin_amdgcn_permlane32_swap(__float_as_uint(m), __float_as_uint(m), false, false); return __uint_as_float(rr[0]) + __uint_as_float(rr[1]); }

template <int CTRL, int RM> DI float dpp_add(float v) { return v + __uint_as_float((unsigned)__builtin_amdgcn_update_dpp(0, (int)__float_as_uint(v), CTRL, RM, 0xf, true)); }
DI float wave_sum(float v) {
    v = dpp_add<0x111, 0xf>(v); v = dpp_add<0x112, 0xf>(v); v = dpp_add<0x114, 0xf>(v); v = dpp_add<0x118, 0xf>(v);
    v = dpp_add<0x142, 0xa>(v); v = dpp_add<0x143, 0xc>(v);
    return __uint_as_float((unsigned)__builtin_amdgcn_readlane((int)__float_as_uint(v), 63));
}
constexpr int K0_LN = SEQ / 4, K0_WS = FF / 64;
__global__ __launch_bounds__(256) void k0_ln(const u16* __restrict__ x, const u16* __restrict__ w1, u16* __restrict__ xn, float* __restrict__ st2, float* __restrict__ w1s, unsigned* __restrict__ cnt) {
    const int lane = threadIdx.x & 63;
    if (blockIdx.x == 0 && threadIdx.x < 160) cnt[threadIdx.x * 32] = 0u;
    if (blockIdx.x >= SEQ / 4) {
        __shared__ float red[32][65];
        const int cg = threadIdx.x & 7, rg = threadIdx.x >> 3, col0 = (blockIdx.x - SEQ / 4) * 64;
        const u32x4* src = (const u32x4*)(w1 + (size_t)(rg * 20) * FF + col0 + cg * 8);
        u32x4 t[20];
#pragma unroll
        for (int i = 0; i < 20; ++i) t[i] = src[(size_t)i * (FF / 8)];
        float a[8] = {0.f, 0.f, 0.f, 0.f, 0.f, 0.f, 0.f, 0.f};
#pragma unroll
        for (int i = 0; i < 20; ++i)
#pragma unroll
            for (int e = 0; e < 4; ++e) { a[2 * e] += __uint_as_float(t[i][e] << 16); a[2 * e + 1] += __uint_as_float(t[i][e] & 0xffff0000u); }
#pragma unroll
        for (int e = 0; e < 8; ++e) red[rg][cg * 8 + e] = a[e];
        __syncthreads();
        if (threadIdx.x < 64) { float s = 0.f;
#pragma unroll
            for (int r = 0; r < 32; ++r) s += red[r][threadIdx.x];
            w1s[col0 + threadIdx.x] = s; }
        return;
    }
    const int row = blockIdx.x * 4 + (threadIdx.x >> 6);
    const u32x4* xr = (const u32x4*)(x + (size_t)row * DM);
    const bool two = lane < 16;
    u32x4 w0 = xr[lane], w1v = two ? xr[64 + lane] : (u32x4){0u, 0u, 0u, 0u};
    float v[16];
#pragma unroll
    for (int i = 0; i < 4; ++i) { v[2 * i] = __uint_as_float(w0[i] << 16); v[2 * i + 1] = __uint_as_float(w0[i] & 0xffff0000u);
                                  v[8 + 2 * i] = __uint_as_float(w1v[i] << 16); v[8 + 2 * i + 1] = __uint_as_float(w1v[i] & 0xffff0000u); }
    const size_t base = (size_t)(row >> 5) * 40 * 64 * 8 + (size_t)(row & 31) * 8;
    const size_t off0 = base + (size_t)(lane >> 1) * 512 + (lane & 1) * 256;
    const size_t off1 = base + (size_t)((64 + lane) >> 1) * 512 + (lane & 1) * 256;
#pragma unroll
    for (int pass = 0; pass < 2; ++pass) {
        float s = 0.f;
#pragma unroll
        for (int i = 0; i < 16; ++i) s += v[i];
        const float mean = wave_sum(s) * (1.0f / DM);
        float q = 0.f;
#pragma unroll
        for (int i = 0; i < 8; ++i) { float d = v[i] - mean; q += d * d; }
        if (two) {
#pragma unroll
            for (int i = 8; i < 16; ++i) { float d = v[i] - mean; q += d * d; }
        }
        const float var = wave_sum(q) * (1.0f / DM);
        const float mb = rbf(mean), vb = rbf(var);
        const float den = rbf(__fsqrt_rn(rbf(vb + 1e-5f)));
        if (pass == 1) { if (lane == 0) { st2[2 * row] = mb; st2[2 * row + 1] = den; } break; }
#pragma unroll
        for (int i = 0; i < 16; ++i) v[i] = rbf(__fdiv_rn(rbf(v[i] - mb), den));
        if (!two) {
#pragma unroll
            for (int i = 8; i < 16; ++i) v[i] = 0.f;
        }
        u32x4 a, b;
#pragma unroll
        for (int i = 0; i < 4; ++i) { a[i] = (__float_as_uint(v[2 * i]) >> 16) | (__float_as_uint(v[2 * i + 1]) & 0xffff0000u);
                                      b[i] = (__float_as_uint(v[8 + 2 * i]) >> 16) | (__float_as_uint(v[8 + 2 * i + 1]) & 0xffff0000u); }
        *(u32x4*)(xn + off0) = a;
        if (two) *(u32x4*)(xn + off1) = b;
    }
}

constexpr int SLOT = 24576, LDS_K = 0, LDS_V = 3 * SLOT, PSLOT = 32768, K1_LDS = 163840;
constexpr float CSC = 0.07216878364870322f * 1.4426950408889634f;
constexpr float THR = 8.f;

DI float gelu_tanh(float x) {
    const float e = __builtin_amdgcn_exp2f(x * __builtin_fmaf(x * x, 0.10294324f, 2.3022082f));
    return __builtin_fmaf(-x, __builtin_amdgcn_rcpf(e + 1.f), x);
}
template <int OFF> DI void gld16(bf16x8& d, unsigned voff, const void* sb) { asm volatile("global_load_dwordx4 %0, %1, %2 offset:%3" : "=v"(d) : "v"(voff), "s"(sb), "i"(OFF) : "memory"); }
template <int OFF> DI void gld16f(f32x4& d, unsigned voff, const void* sb) { asm volatile("global_load_dwordx4 %0, %1, %2 offset:%3" : "=v"(d) : "v"(voff), "s"(sb), "i"(OFF) : "memory"); }
template <int OFF> DI void gld8(u32x2& d, unsigned voff, const void* sb) { asm volatile("global_load_dwordx2 %0, %1, %2 offset:%3" : "=v"(d) : "v"(voff), "s"(sb), "i"(OFF) : "memory"); }

template <int I> DI bf16x8 pfrag(lds_cptr wqp, lds_cptr w1p) {
    constexpr int s = I >> 3, j = I & 7;
    if constexpr (j < 6) { constexpr int off = 2 * s * 3072 + (j >> 1) * 1024 + (j & 1) * 512; return cat(vtr(wqp + off), vtr(wqp + off + 256)); }
    else                 { constexpr int off = 2 * s * 1024 + (j - 6) * 512; return cat(vtr(w1p + off), vtr(w1p + off + 256)); }
}
template <int I, bool NEXT, class H> DI void pstep(f32x16 (&o6)[6], f32x16 (&hacc)[2], bf16x8 (&f)[4], const bf16x8 (&xf)[4], lds_cptr wqp, lds_cptr w1p, lds_cptr wqn, lds_cptr w1n, H&& hook) {
    constexpr int s = I >> 3, j = I & 7;
    if constexpr (I + 3 < 32) f[(I + 3) & 3] = pfrag<I + 3>(wqp, w1p);
    else if constexpr (NEXT) f[(I + 3) & 3] = pfrag<I + 3 - 32>(wqn, w1n);
    if constexpr (j < 6) { o6[j] = MFMA(f[I & 3], xf[s], o6[j]); PIN(o6[j]); } else { hacc[j - 6] = MFMA(f[I & 3], xf[s], hacc[j - 6]); PIN(hacc[j - 6]); }
    hook(std::integral_constant<int, I>{});
    asm volatile("" ::: "memory"); SBAR();
    if constexpr (I + 1 < 32) pstep<I + 1, NEXT>(o6, hacc, f, xf, wqp, w1p, wqn, w1n, hook);
}

__global__ __launch_bounds__(512, 2)
void k1_attn(const u16* __restrict__ xn, const float* __restrict__ st2, const float* __restrict__ w1sum, const u16* __restrict__ Kc, const u16* __restrict__ Vc,
             const u16* __restrict__ wq, const u16* __restrict__ w1, const u16* __restrict__ b1,
             u16* __restrict__ o, u16* __restrict__ hbuf, const u16* __restrict__ wo, const u16* __restrict__ w2, u16* __restrict__ wpk
             ) {
    extern __shared__ __attribute__((aligned(16))) char lds[];
    const int tid = threadIdx.x, lane = tid & 63, r32 = lane & 31, hi = lane >> 5;
    const int wid = __builtin_amdgcn_readfirstlane(tid >> 6);
    const int bid = blockIdx.x, xcd = bid & 7, jj = bid >> 3;
    const int head = xcd + 8 * (jj >> 3), qb = jj & 7;
    const int row0 = qb * 256 + wid * 32;
    const unsigned lds0 = (unsigned)(uintptr_t)lds;
    const lds_cptr L = (lds_cptr)lds;

    const int vd1 = lane >> 5, vr7 = (lane >> 2) & 7, vch = lane & 3;
    const int kr7 = ((lane >> 4) << 1) | (((lane >> 3) & 1) ^ ((wid >> 1) & 1)), kc8 = (lane & 7) ^ kr7;
    const int q4 = (lane & 15) >> 2, p4 = lane & 3, g1 = (lane >> 4) & 1;
    const int r7 = r32 & 7;

    const char* kbase = (const char*)(Kc + (size_t)head * SEQ * DH);
    const char* vbase = (const char*)(Vc + (size_t)head * SEQ * DH);
    const unsigned kvo = kr7 * (DH * 2) + kc8 * 16, vvo = vr7 * (DH * 2) + vd1 * 64 + vch * 16;
    const unsigned kdst = lds0 + LDS_K + wid * 3072, vdst = lds0 + LDS_V + wid * 3072;
#define DMA_K(t, slot) do { const char* s_ = kbase + (size_t)((64 * (t) + 32 + 8 * wid) & (SEQ - 1)) * (DH * 2); \
        gldss(kvo, s_, kdst + (slot)); gldss(kvo, s_ + 128, kdst + (slot) + 1024); gldss(kvo, s_ + 256, kdst + (slot) + 2048); } while (0)
#define DMA_V(t, slot) do { const char* s_ = vbase + (size_t)((64 * (t) + 8 * wid) & (SEQ - 1)) * (DH * 2); \
        gldss(vvo, s_, vdst + (slot)); gldss(vvo, s_ + 128, vdst + (slot) + 1024); gldss(vvo, s_ + 256, vdst + (slot) + 2048); } while (0)

    f32x16 o6[6];
    f32x16 hacc[2];
#pragma unroll
    for (int i = 0; i < 6; ++i) o6[i] = f32x16{};
    hacc[0] = f32x16{}; hacc[1] = f32x16{};
    const int fcol0 = (48 * head < FF - 64) ? 48 * head : FF - 64;
    u32x2 bb[8], stv; f32x4 wsv[8];
    {
        const char* wqs = (const char*)(wq + (size_t)(8 * wid) * DQ + head * DH);
        const char* w1s = (const char*)(w1 + (size_t)(8 * wid) * FF + fcol0);
        const unsigned wqo = vr7 * (DQ * 2) + vd1 * 64 + vch * 16, w1o = vr7 * (FF * 2) + vd1 * 64 + vch * 16;
        const unsigned wqd = lds0 + wid * 3072, w1d = lds0 + 24576 + wid * 1024;
#define CSRC(c) (c)
#define DMA_W(c) do { \
            gldss(wqo, wqs + (size_t)(c) * 64 * DQ * 2,       wqd + ((c) % 5) * PSLOT); \
            gldss(wqo, wqs + (size_t)(c) * 64 * DQ * 2 + 128, wqd + ((c) % 5) * PSLOT + 1024); \
            gldss(wqo, wqs + (size_t)(c) * 64 * DQ * 2 + 256, wqd + ((c) % 5) * PSLOT + 2048); \
            gldss(w1o, w1s + (size_t)(c) * 64 * FF * 2,       w1d + ((c) % 5) * PSLOT); } while (0)
        const int p4s = ((p4 & 1) << 1) | (p4 >> 1);
        const lds_cptr wqp = L + hi * 3072 + q4 * 64 + g1 * 32 + p4s * 8;
        const lds_cptr w1p = L + 24576 + hi * 1024 + q4 * 64 + g1 * 32 + p4 * 8;
        const char* xsrc = (const char*)(xn + (size_t)(row0 >> 5) * 40 * 512);
        const char* bsrc = (const char*)(b1 + fcol0);
        const char* wssrc = (const char*)(w1sum + fcol0);
        const char* stsrc = (const char*)(st2 + 2 * row0);
        const unsigned xo = lane * 16, bo = 8 * hi, wso = 16 * hi, sto = 8 * r32;
        bf16x8 xa[4], xb[4], f[4];
#define LDX(c, X) do { const char* xp_ = xsrc + CSRC(c) * 4096; \
            gld16<0>(X[0], xo, xp_); gld16<1024>(X[1], xo, xp_); gld16<2048>(X[2], xo, xp_); gld16<3072>(X[3], xo, xp_); } while (0)
#define WAITX(N, X) asm volatile("s_waitcnt vmcnt(" #N ") lgkmcnt(0)\n\ts_barrier" : "+v"(X[0]), "+v"(X[1]), "+v"(X[2]), "+v"(X[3]) :: "memory")
#define PCOMP(c, X, PRE, NEXT, HOOK) do { lds_cptr a_ = wqp + ((c) % 5) * PSLOT; lds_cptr b_ = w1p + ((c) % 5) * PSLOT; lds_cptr an_ = wqp + (((c) + 1) % 5) * PSLOT; lds_cptr bn_ = w1p + (((c) + 1) % 5) * PSLOT; \
            PIN(a_); PIN(b_); PIN(an_); PIN(bn_); \
            if (!(PRE)) { f[0] = pfrag<0>(a_, b_); f[1] = pfrag<1>(a_, b_); f[2] = pfrag<2>(a_, b_); } SBAR(); \
            pstep<0, NEXT>(o6, hacc, f, X, a_, b_, an_, bn_, HOOK); } while (0)
#define XHOOK(cn, XN) if constexpr (I == 1) gld16<0>(XN[0], xo, xsrc + (cn) * 4096); if constexpr (I == 4) gld16<1024>(XN[1], xo, xsrc + (cn) * 4096); \
                      if constexpr (I == 7) gld16<2048>(XN[2], xo, xsrc + (cn) * 4096); if constexpr (I == 10) gld16<3072>(XN[3], xo, xsrc + (cn) * 4096);
#define WHOOK(cw) if constexpr (I == 13) gldss(wqo, wqs + (size_t)(cw) * 64 * DQ * 2,       wqd + ((cw) % 5) * PSLOT); \
                  if constexpr (I == 17) gldss(wqo, wqs + (size_t)(cw) * 64 * DQ * 2 + 128, wqd + ((cw) % 5) * PSLOT + 1024); \
                  if constexpr (I == 21) gldss(wqo, wqs + (size_t)(cw) * 64 * DQ * 2 + 256, wqd + ((cw) % 5) * PSLOT + 2048); \
                  if constexpr (I == 25) gldss(w1o, w1s + (size_t)(cw) * 64 * FF * 2,       w1d + ((cw) % 5) * PSLOT);
#define KHOOK(t, slot, I0) { const char* s_ = kbase + (size_t)((64 * (t) + 32 + 8 * wid) & (SEQ - 1)) * (DH * 2); \
                  if constexpr (I == (I0)) gldss(kvo, s_, kdst + (slot)); if constexpr (I == (I0) + 3) gldss(kvo, s_ + 128, kdst + (slot) + 1024); if constexpr (I == (I0) + 6) gldss(kvo, s_ + 256, kdst + (slot) + 2048); }
#define VHOOK(t, slot, I0) { const char* s_ = vbase + (size_t)((64 * (t) + 8 * wid) & (SEQ - 1)) * (DH * 2); \
                  if constexpr (I == (I0)) gldss(vvo, s_, vdst + (slot)); if constexpr (I == (I0) + 3) gldss(vvo, s_ + 128, vdst + (slot) + 1024); if constexpr (I == (I0) + 6) gldss(vvo, s_ + 256, vdst + (slot) + 2048); }
#define HK(body) [&](auto Ic) { constexpr int I = decltype(Ic)::value; body }
        LDX(0, xa); DMA_W(0); DMA_W(1); DMA_W(2); DMA_W(3);
        WAITX(12, xa); PCOMP(0, xa, false, false, HK(XHOOK(1, xb) WHOOK(4)));
        WAITX(4, xb);  PCOMP(1, xb, false, true,  HK(XHOOK(2, xa) WHOOK(5)));
        WAITX(4, xa);  PCOMP(2, xa, true,  true,  HK(XHOOK(3, xb) WHOOK(6)));
        WAITX(4, xb);  PCOMP(3, xb, true,  true,  HK(XHOOK(4, xa) WHOOK(7)));
        WAITX(4, xa);  PCOMP(4, xa, true,  true,  HK(XHOOK(5, xb) WHOOK(8)));
        WAITX(4, xb);  PCOMP(5, xb, true,  true,  HK(XHOOK(6, xa) WHOOK(9)));
        WAITX(4, xa);  PCOMP(6, xa, true,  true,  HK(XHOOK(7, xb) KHOOK(-1, 0, 13)));
        WAITX(3, xb);  PCOMP(7, xb, true,  true,  HK(XHOOK(8, xa) KHOOK(0, SLOT, 13)));
        WAITX(3, xa);  PCOMP(8, xa, true,  true,  HK(XHOOK(9, xb) KHOOK(1, 2 * SLOT, 13) VHOOK(0, 0, 22)));
        WAITX(6, xb);  PCOMP(9, xb, true,  false, HK(
            if constexpr (I == 1) { gld8<0>(bb[0], bo, bsrc); gld8<16>(bb[1], bo, bsrc); } if constexpr (I == 2) { gld8<32>(bb[2], bo, bsrc); gld8<48>(bb[3], bo, bsrc); }
            if constexpr (I == 3) { gld8<64>(bb[4], bo, bsrc); gld8<80>(bb[5], bo, bsrc); } if constexpr (I == 4) { gld8<96>(bb[6], bo, bsrc); gld8<112>(bb[7], bo, bsrc); }
            if constexpr (I == 5) { gld16f<0>(wsv[0], wso, wssrc); gld16f<32>(wsv[1], wso, wssrc); } if constexpr (I == 6) { gld16f<64>(wsv[2], wso, wssrc); gld16f<96>(wsv[3], wso, wssrc); }
            if constexpr (I == 7) { gld16f<128>(wsv[4], wso, wssrc); gld16f<160>(wsv[5], wso, wssrc); } if constexpr (I == 8) { gld16f<192>(wsv[6], wso, wssrc); gld16f<224>(wsv[7], wso, wssrc); }
            if constexpr (I == 9) gld8<0>(stv, sto, stsrc);
            VHOOK(1, SLOT, 13)));
        asm volatile("s_waitcnt vmcnt(3)" : "+v"(bb[0]), "+v"(bb[1]), "+v"(bb[2]), "+v"(bb[3]), "+v"(bb[4]), "+v"(bb[5]), "+v"(bb[6]), "+v"(bb[7]),
                     "+v"(wsv[0]), "+v"(wsv[1]), "+v"(wsv[2]), "+v"(wsv[3]), "+v"(wsv[4]), "+v"(wsv[5]), "+v"(wsv[6]), "+v"(wsv[7]), "+v"(stv) :: "memory");
#undef DMA_W
#undef LDX
#undef WAITX
#undef PCOMP
    }
    bf16x8 qf[12];
#pragma unroll
    for (int db = 0; db < 6; ++db)
#pragma unroll
        for (int s = 0; s < 2; ++s) {
            u32x4 w;
#pragma unroll
            for (int e = 0; e < 4; ++e) w[e] = cvtpk(o6[db][8 * s + 2 * e] * CSC, o6[db][8 * s + 2 * e + 1] * CSC);
            qf[2 * db + s] = __builtin_bit_cast(bf16x8, w);
        }
    {
        const int flo = 48 * head - fcol0;
        const float m2 = __uint_as_float(stv[0]), rden = __fdiv_rn(1.0f, __uint_as_float(stv[1]));
        char* hbase = (char*)hbuf + (size_t)(row0 >> 5) * 96 * 1024 + r32 * 16 + 8 * hi;
#pragma unroll
        for (int ft = 0; ft < 2; ++ft)
#pragma unroll
            for (int g = 0; g < 4; ++g) {
                const int fl = 32 * ft + 8 * g;
                if (fl >= flo && fl < flo + 48) {
                    float hv[4];
#pragma unroll
                    for (int e = 0; e < 4; ++e) {
                        const unsigned bw = bb[4 * ft + g][e >> 1];
                        const float bias = (e & 1) ? __uint_as_float(bw & 0xffff0000u) : __uint_as_float(bw << 16);
                        hv[e] = gelu_tanh(rbf(rbf(__builtin_fmaf(-m2, wsv[4 * ft + g][e], hacc[ft][4 * g + e]) * rden) + bias));
                    }
                    u32x2 w; w[0] = cvtpk(hv[0], hv[1]); w[1] = cvtpk(hv[2], hv[3]);
                    const int fc = (fcol0 + fl) >> 3;
                    *(u32x2*)(hbase + (fc >> 1) * 1024 + (fc & 1) * 512) = w;
                }
            }
    }
#pragma unroll
    for (int i = 0; i < 6; ++i) o6[i] = f32x16{};

    lds_cptr kp[4];
#pragma unroll
    for (int j = 0; j < 4; ++j)
        kp[j] = L + LDS_K + (r32 >> 3) * 3072 + ((r7 >> 1) << 8) + ((((r32 >> 4) & 1) ^ (r7 & 1)) << 7) + ((r7 ^ (2 * j + hi)) << 4);
    const lds_cptr vp = L + LDS_V + (4 * hi + q4) * 64 + g1 * 32 + p4 * 8;

    f32x16 SA, SB, NEGM; float mref, lsum = 0.f;
    u32x4 P0 = {0u,0u,0u,0u}, P1 = {0u,0u,0u,0u};
#define KRD(sl_, half, ks) ldsr(kp[(ks) & 3] + (sl_) + (half) * 12288 + ((ks) >> 2) * 1024)
#define VRD(sl_, half, s, db) cat(vtr(vp + (sl_) + (4 * (half) + 2 * (s)) * 3072 + ((db) >> 1) * 1024 + ((db) & 1) * 512), \
                                  vtr(vp + (sl_) + (4 * (half) + 2 * (s) + 1) * 3072 + ((db) >> 1) * 1024 + ((db) & 1) * 512))
    {
        SA = f32x16{};
#pragma unroll
        for (int ks = 0; ks < 12; ++ks) SA = MFMA(KRD(0, 1, ks), qf[ks], SA);
        float mx = SA[0];
#pragma unroll
        for (int i = 1; i < 16; ++i) mx = __builtin_fmaxf(mx, SA[i]);
        mref = xhalf_max(mx);
#pragma unroll
        for (int i = 0; i < 16; ++i) { SA[i] -= mref; NEGM[i] = -mref; }
    }
    WAIT_BAR(3);

#define EX(v) __builtin_amdgcn_exp2f(v)
#define MX3(a, b, c) max3a((a), (b), (c))
#define GEND() do { asm volatile("" ::: "memory"); SBAR(); } while (0)
#define HSTEP(SC, SN, ksl, khalf, vsl, vhalf, nksl, nkhalf, D0, D1, D2, D3, D4, D5) do { \
        float sa = 0.f, sb = 0.f, mx = 0.f; \
        k3 = KRD(ksl, khalf, 3);  SN = MFMA(k0, qf[0], NEGM); PIN(SN); float p0 = EX(SC[0]); PIN(p0); GEND(); \
        k0 = KRD(ksl, khalf, 4);  SN = MFMA(k1, qf[1], SN); PIN(SN); D0; float p1 = EX(SC[1]); PIN(p1); GEND(); \
        k1 = KRD(ksl, khalf, 5);  SN = MFMA(k2, qf[2], SN); PIN(SN); float p2 = EX(SC[2]); P0[0] = cvtpk(p0, p1); PIN(p2); PIN(P0); GEND(); \
        k2 = KRD(ksl, khalf, 6);  SN = MFMA(k3, qf[3], SN); PIN(SN); float p3 = EX(SC[3]); sa = p0 + p1; PIN(p3); PIN(sa); GEND(); \
        k3 = KRD(ksl, khalf, 7);  SN = MFMA(k0, qf[4], SN); PIN(SN); float p4 = EX(SC[4]); P0[1] = cvtpk(p2, p3); PIN(p4); PIN(P0); GEND(); \
        k0 = KRD(ksl, khalf, 8);  SN = MFMA(k1, qf[5], SN); PIN(SN); D1; float p5 = EX(SC[5]); sb = p2 + p3; PIN(p5); PIN(sb); GEND(); \
        k1 = KRD(ksl, khalf, 9);  SN = MFMA(k2, qf[6], SN); PIN(SN); float p6 = EX(SC[6]); P0[2] = cvtpk(p4, p5); PIN(p6); PIN(P0); GEND(); \
        k2 = KRD(ksl, khalf, 10); SN = MFMA(k3, qf[7], SN); PIN(SN); float p7 = EX(SC[7]); sa += p4; sb += p5; PIN(p7); PIN(sa); PIN(sb); GEND(); \
        k3 = KRD(ksl, khalf, 11); SN = MFMA(k0, qf[8], SN); PIN(SN); float p8 = EX(SC[8]); P0[3] = cvtpk(p6, p7); PIN(p8); PIN(P0); GEND(); \
        bf16x8 v0 = VRD(vsl, vhalf, 0, 0); SN = MFMA(k1, qf[9], SN); PIN(SN); D2; float p9 = EX(SC[9]); sa += p6; sb += p7; PIN(p9); PIN(sa); PIN(sb); GEND(); \
        bf16x8 v1 = VRD(vsl, vhalf, 0, 1); SN = MFMA(k2, qf[10], SN); PIN(SN); float p10 = EX(SC[10]); P1[0] = cvtpk(p8, p9); PIN(p10); PIN(P1); GEND(); \
        bf16x8 v2 = VRD(vsl, vhalf, 0, 2); SN = MFMA(k3, qf[11], SN); PIN(SN); float p11 = EX(SC[11]); sa += p8; sb += p9; PIN(p11); PIN(sa); PIN(sb); GEND(); \
        const bf16x8 pa0 = __builtin_bit_cast(bf16x8, P0); \
        bf16x8 v3; \
        v3 = VRD(vsl, vhalf, 0, 3); o6[0] = MFMA(v0, pa0, o6[0]); PIN(o6[0]); float p12 = EX(SC[12]); P1[1] = cvtpk(p10, p11); PIN(p12); PIN(P1); GEND(); \
        v0 = VRD(vsl, vhalf, 0, 4); o6[1] = MFMA(v1, pa0, o6[1]); PIN(o6[1]); D3; float p13 = EX(SC[13]); sa += p10; sb += p11; PIN(p13); PIN(sa); PIN(sb); GEND(); \
        v1 = VRD(vsl, vhalf, 0, 5); o6[2] = MFMA(v2, pa0, o6[2]); PIN(o6[2]); float p14 = EX(SC[14]); P1[2] = cvtpk(p12, p13); PIN(p14); PIN(P1); GEND(); \
        v2 = VRD(vsl, vhalf, 1, 0); o6[3] = MFMA(v3, pa0, o6[3]); PIN(o6[3]); float p15 = EX(SC[15]); sa += p12; sb += p13; PIN(p15); PIN(sa); PIN(sb); GEND(); \
        v3 = VRD(vsl, vhalf, 1, 1); o6[4] = MFMA(v0, pa0, o6[4]); PIN(o6[4]); P1[3] = cvtpk(p14, p15); mx = MX3(SN[0], SN[1], SN[2]); mx = MX3(mx, SN[3], SN[4]); PIN(P1); PIN(mx); GEND(); \
        v0 = VRD(vsl, vhalf, 1, 2); o6[5] = MFMA(v1, pa0, o6[5]); PIN(o6[5]); D4; sa += p14; sb += p15; mx = MX3(mx, SN[5], SN[6]); PIN(sa); PIN(sb); PIN(mx); GEND(); \
        const bf16x8 pa1 = __builtin_bit_cast(bf16x8, P1); \
        v1 = VRD(vsl, vhalf, 1, 3); o6[0] = MFMA(v2, pa1, o6[0]); PIN(o6[0]); mx = MX3(mx, SN[7], SN[8]); lsum += sa + sb; PIN(mx); PIN(lsum); GEND(); \
        v2 = VRD(vsl, vhalf, 1, 4); o6[1] = MFMA(v3, pa1, o6[1]); PIN(o6[1]); mx = MX3(mx, SN[9], SN[10]); mx = MX3(mx, SN[11], SN[12]); PIN(mx); GEND(); \
        v3 = VRD(vsl, vhalf, 1, 5); o6[2] = MFMA(v0, pa1, o6[2]); PIN(o6[2]); mx = MX3(mx, SN[13], SN[14]); mx = max3a(mx, SN[15], SN[15]); PIN(mx); GEND(); \
        k0 = KRD(nksl, nkhalf, 0);  o6[3] = MFMA(v1, pa1, o6[3]); PIN(o6[3]); D5; GEND(); \
        k1 = KRD(nksl, nkhalf, 1);  o6[4] = MFMA(v2, pa1, o6[4]); PIN(o6[4]); GEND(); \
        k2 = KRD(nksl, nkhalf, 2);  o6[5] = MFMA(v3, pa1, o6[5]); PIN(o6[5]); GEND(); \
        if (__builtin_expect(__any(mx > THR), 0)) { \
            const float dl = __builtin_fmaxf(xhalf_max(mx), 0.f); mref += dl; const float f = __builtin_amdgcn_exp2f(-dl); lsum *= f; \
            _Pragma("unroll") for (int r_ = 0; r_ < 16; ++r_) { SN[r_] -= dl; NEGM[r_] = -mref; } \
            _Pragma("unroll") for (int d_ = 0; d_ < 6; ++d_) _Pragma("unroll") for (int r_ = 0; r_ < 16; ++r_) o6[d_][r_] *= f; \
        } \
    } while (0)

    bf16x8 k0 = KRD(SLOT, 0, 0), k1 = KRD(SLOT, 0, 1), k2 = KRD(SLOT, 0, 2), k3;
    u32x4 pkv = {0u, 0u, 0u, 0u};
    const int pk0 = (bid * 8 + wid) * 5;
    const unsigned pko = vr7 * (DM * 2) + vd1 * 64 + vch * 16, xo16 = lane * 16;
    int s0 = 0;
    auto pk_store = [&](int t) { const int P = (pk0 + t - 1 < 9600) ? pk0 + t - 1 : 9599;
        if (t >= 1) asm volatile("s_waitcnt vmcnt(6)\n\tglobal_store_dwordx4 %0, %1, %2\n\ts_nop 1" :: "v"(xo16), "v"(pkv), "s"((const char*)wpk + (size_t)P * 1024) : "memory"); };
    auto pk_load = [&](int t) { const int P = (pk0 + t < 9600) ? pk0 + t : 9599, b = P >> 4, tn = b / 120, g = b - tn * 120;
        const char* src = ((g < 96) ? (const char*)(wo + (size_t)g * 64 * DM) : (const char*)(w2 + (size_t)(g - 96) * 64 * DM)) + (size_t)(8 * ((P >> 1) & 7)) * (DM * 2) + tn * 256 + (P & 1) * 128;
        if (t < 5) asm volatile("global_load_dwordx4 %0, %1, %2" : "+v"(pkv) : "v"(pko), "s"(src) : "memory"); };
#define ITER(t, E0, E1, WAITN) do { \
        const int s1 = (s0 == 2 * SLOT) ? 0 : s0 + SLOT;         \
        const int s2 = (s0 == 0) ? 2 * SLOT : s0 - SLOT;         \
        const char* ks_ = kbase + (size_t)((64 * ((t) + 2) + 32 + 8 * wid) & (SEQ - 1)) * (DH * 2); \
        const char* vs_ = vbase + (size_t)((64 * ((t) + 2) + 8 * wid) & (SEQ - 1)) * (DH * 2); \
        HSTEP(SA, SB, s1, 0, s0, 0, s1, 1, gldss(kvo, ks_, kdst + s0), gldss(kvo, ks_ + 128, kdst + s0 + 1024), gldss(kvo, ks_ + 256, kdst + s0 + 2048), \
              gldss(vvo, vs_, vdst + s2), gldss(vvo, vs_ + 128, vdst + s2 + 1024), gldss(vvo, vs_ + 256, vdst + s2 + 2048)); \
        HSTEP(SB, SA, s1, 1, s0, 1, s2, 0, (void)0, (void)0, E0, E1, (void)0, (void)0); \
        WAIT_BAR(WAITN); \
        s0 = s1; } while (0)
    for (int t = 0; t < 6; ++t) ITER(t, pk_store(t), pk_load(t), 3);
    for (int t = 6; t < 32; ++t) ITER(t, (void)0, (void)0, 3);
    asm volatile("s_waitcnt vmcnt(0)" ::: "memory");
    {
        const float il = __builtin_amdgcn_rcpf(xhalf_sum(lsum));
        char* obase = (char*)o + ((size_t)(row0 >> 5) * 384 + head * 12) * 1024 + r32 * 16 + 8 * hi;
#pragma unroll
        for (int db = 0; db < 6; ++db)
#pragma unroll
            for (int g = 0; g < 4; ++g) {
                u32x2 w; w[0] = cvtpk(o6[db][4 * g] * il, o6[db][4 * g + 1] * il); w[1] = cvtpk(o6[db][4 * g + 2] * il, o6[db][4 * g + 3] * il);
                *(u32x2*)(obase + (2 * db + (g >> 1)) * 1024 + (g & 1) * 512) = w;
            }
    }
}

constexpr int K2_STAGE = 16384, K2_LDS = 8 * K2_STAGE;
DI void st16_sc1(const void* sb, unsigned voff, f32x4 v) { asm volatile("global_store_dwordx4 %0, %1, %2 sc1\n\ts_nop 1" :: "v"(voff), "v"(v), "s"(sb) : "memory"); }
template <int OFF> DI void ld16_sc1(f32x4& d, unsigned voff, const void* sb) { asm volatile("global_load_dwordx4 %0, %1, %2 offset:%3 sc1" : "=v"(d) : "v"(voff), "s"(sb), "i"(OFF) : "memory"); }
__global__ __launch_bounds__(512, 2)
void k2_gemm(const u16* __restrict__ o, const u16* __restrict__ hbuf, const u16* __restrict__ wpk, float* __restrict__ part, unsigned* __restrict__ cnt,
             const u16* __restrict__ x, const u16* __restrict__ b2, u16* __restrict__ out
             ) {
    extern __shared__ __attribute__((aligned(16))) char lds[];
    const int tid = threadIdx.x, lane = tid & 63, r32 = lane & 31, hi = lane >> 5;
    const int wid = __builtin_amdgcn_readfirstlane(tid >> 6), wm = wid & 3, kh = wid >> 2;
    const int bid = blockIdx.x;
    const int lin = (bid & 7) * 30 + (bid >> 3);
    const int chunk = lin / 80, tile = lin - chunk * 80, tm = tile / 5, tn = tile - tm * 5;
    const unsigned lds0 = (unsigned)(uintptr_t)lds;
    const lds_cptr L = (lds_cptr)lds;
    const char* Ao = (const char*)o + ((size_t)(4 * tm + wm) * 384 + 2 * kh) * 1024;
    const char* Ah = (const char*)hbuf + ((size_t)(4 * tm + wm) * 96 + 2 * kh) * 1024;
    const char* Wp = (const char*)wpk + (size_t)tn * 120 * 16384 + wid * 2048;
    const unsigned avo = lane * 16;
    const unsigned wdst = lds0 + wid * 2048;
    const int g0 = chunk * 40;
    const int q4 = (lane & 15) >> 2, p4 = lane & 3, g1 = (lane >> 4) & 1;
    const lds_cptr wp = L + (4 * kh + hi) * 2048 + q4 * 64 + g1 * 32 + p4 * 8;
    f32x16 acc[4], sv[4];
#pragma unroll
    for (int i = 0; i < 4; ++i) acc[i] = f32x16{};
    bf16x8 ab[8][2];
#define SRC2(st) const int g_ = g0 + (((st) < 40) ? (st) : 39); const char* w_ = Wp + (size_t)g_ * 16384; \
        const char* a_ = (g_ < 96) ? Ao + (size_t)g_ * 4096 : Ah + (size_t)(g_ - 96) * 4096;
#define ISSUE2(st, sl) do { SRC2(st); gld16<0>(ab[sl][0], avo, a_); gld16<1024>(ab[sl][1], avo, a_); \
        gldss(avo, w_, wdst + (sl) * K2_STAGE); gldss(avo, w_ + 1024, wdst + (sl) * K2_STAGE + 1024); } while (0)
    ISSUE2(0, 0); ISSUE2(1, 1); ISSUE2(2, 2); ISSUE2(3, 3); ISSUE2(4, 4); ISSUE2(5, 5);
#define WF(sl, s, ni) cat(vtr(wq_[(sl) >> 1] + ((sl) & 1) * K2_STAGE + (s) * 4096 + ((ni) >> 1) * 1024 + ((ni) & 1) * 512), vtr(wq_[(sl) >> 1] + ((sl) & 1) * K2_STAGE + (s) * 4096 + ((ni) >> 1) * 1024 + ((ni) & 1) * 512 + 256))
#define NS(sl) (((sl) + 6) & 7)
#define N1(sl) (((sl) + 1) & 7)
    lds_cptr wq_[4];
#pragma unroll
    for (int i = 0; i < 4; ++i) { wq_[i] = wp + 2 * i * K2_STAGE; PIN(wq_[i]); }
#define STAGE2(st, sl) do { \
        asm volatile("s_waitcnt vmcnt(16) lgkmcnt(0)\n\ts_barrier" : "+v"(ab[sl][0]), "+v"(ab[sl][1]) :: "memory"); \
        SRC2((st) + 6); \
        f3 = WF(sl, 0, 3); acc[0] = MFMA(f0, ab[sl][0], acc[0]); PIN(acc[0]); gld16<0>(ab[NS(sl)][0], avo, a_); GEND2(); \
        f0 = WF(sl, 1, 0); acc[1] = MFMA(f1, ab[sl][0], acc[1]); PIN(acc[1]); gld16<1024>(ab[NS(sl)][1], avo, a_); GEND2(); \
        f1 = WF(sl, 1, 1); acc[2] = MFMA(f2, ab[sl][0], acc[2]); PIN(acc[2]); gldss(avo, w_, wdst + NS(sl) * K2_STAGE); GEND2(); \
        f2 = WF(sl, 1, 2); acc[3] = MFMA(f3, ab[sl][0], acc[3]); PIN(acc[3]); GEND2(); \
        f3 = WF(sl, 1, 3); acc[0] = MFMA(f0, ab[sl][1], acc[0]); PIN(acc[0]); gldss(avo, w_ + 1024, wdst + NS(sl) * K2_STAGE + 1024); GEND2(); \
        f0 = WF(N1(sl), 0, 0); acc[1] = MFMA(f1, ab[sl][1], acc[1]); PIN(acc[1]); GEND2(); \
        f1 = WF(N1(sl), 0, 1); acc[2] = MFMA(f2, ab[sl][1], acc[2]); PIN(acc[2]); GEND2(); \
        f2 = WF(N1(sl), 0, 2); acc[3] = MFMA(f3, ab[sl][1], acc[3]); PIN(acc[3]); GEND2(); \
    } while (0)
#define GEND2() do { asm volatile("" ::: "memory"); SBAR(); } while (0)
    asm volatile("s_waitcnt vmcnt(20)\n\ts_barrier" ::: "memory");
    bf16x8 f0 = WF(0, 0, 0), f1 = WF(0, 0, 1), f2 = WF(0, 0, 2), f3;
#define STAGE8(s8) do { STAGE2(s8, 0); STAGE2(s8 + 1, 1); STAGE2(s8 + 2, 2); STAGE2(s8 + 3, 3); STAGE2(s8 + 4, 4); STAGE2(s8 + 5, 5); STAGE2(s8 + 6, 6); STAGE2(s8 + 7, 7); } while (0)
    for (int s8 = 0; s8 < 16; s8 += 8) STAGE8(s8);
#pragma unroll
    for (int i = 0; i < 4; ++i) { sv[i] = acc[i]; if (chunk == 2) acc[i] = f32x16{}; }
    for (int s8 = 16; s8 < 40; s8 += 8) STAGE8(s8);
    asm volatile("s_waitcnt vmcnt(0) lgkmcnt(0)\n\ts_barrier" : "+v"(ab[0][0]), "+v"(ab[0][1]), "+v"(ab[1][0]), "+v"(ab[1][1]), "+v"(ab[2][0]), "+v"(ab[2][1]),
                 "+v"(ab[3][0]), "+v"(ab[3][1]), "+v"(ab[4][0]), "+v"(ab[4][1]), "+v"(ab[5][0]), "+v"(ab[5][1]), "+v"(f0), "+v"(f1), "+v"(f2) :: "memory");
    char* pbase = (char*)part + (size_t)tile * 4 * 65536 + wid * 8192;
    float* xw = (float*)(lds + wid * 8192); const float* xr = (const float*)(lds + (wid ^ 4) * 8192);
    auto exchange = [&](const f32x16 (&ac)[4], f32x4 (&res)[8]) {
#pragma unroll
        for (int ni = 0; ni < 2; ++ni)
#pragma unroll
            for (int g = 0; g < 4; ++g) { const f32x16& a_ = kh ? ac[ni] : ac[2 + ni]; f32x4 v = {a_[4 * g], a_[4 * g + 1], a_[4 * g + 2], a_[4 * g + 3]}; *(f32x4*)(xw + ((ni * 4 + g) * 64 + lane) * 4) = v; }
        asm volatile("s_waitcnt vmcnt(0) lgkmcnt(0)\n\ts_barrier" ::: "memory");
#pragma unroll
        for (int ni = 0; ni < 2; ++ni)
#pragma unroll
            for (int g = 0; g < 4; ++g) { const f32x16& a_ = kh ? ac[2 + ni] : ac[ni];
                const f32x4 o_ = *(const f32x4*)(xr + ((ni * 4 + g) * 64 + lane) * 4);
                f32x4 v;
                if (kh) { v[0] = o_[0] + a_[4 * g]; v[1] = o_[1] + a_[4 * g + 1]; v[2] = o_[2] + a_[4 * g + 2]; v[3] = o_[3] + a_[4 * g + 3]; }
                else    { v[0] = a_[4 * g] + o_[0]; v[1] = a_[4 * g + 1] + o_[1]; v[2] = a_[4 * g + 2] + o_[2]; v[3] = a_[4 * g + 3] + o_[3]; }
                res[ni * 4 + g] = v; }
        asm volatile("s_waitcnt lgkmcnt(0)\n\ts_barrier" ::: "memory");
    };
    f32x4 ra[8], rb[8];
    if (chunk == 2) { exchange(sv, ra); exchange(acc, rb); } else exchange(acc, ra);
    const int row = tm * 128 + wm * 32 + r32, col0 = tn * 128 + kh * 64 + 4 * hi;
    auto finish = [&](auto CH) {
        constexpr int C = decltype(CH)::value;
        constexpr int NU = (C == 2) ? 4 : 2, U0 = 2 * C;
#pragma unroll
        for (int i = 0; i < 8; ++i) if (i < U0 || i >= U0 + NU) st16_sc1(pbase + (size_t)C * 65536 + i * 1024, avo, ra[i]);
        if constexpr (C == 2) {
#pragma unroll
            for (int i = 0; i < 4; ++i) st16_sc1(pbase + (size_t)3 * 65536 + i * 1024, avo, rb[i]);
        }
        u32x2 xb[NU], bb[NU];
#pragma unroll
        for (int u = 0; u < NU; ++u) { const int i = U0 + u; xb[u] = *(const u32x2*)(x + (size_t)row * DM + col0 + 32 * (i >> 2) + 8 * (i & 3)); bb[u] = *(const u32x2*)(b2 + col0 + 32 * (i >> 2) + 8 * (i & 3)); }
        asm volatile("s_waitcnt vmcnt(0)\n\ts_barrier" ::: "memory");
        if (tid == 0) {
            __hip_atomic_fetch_add(cnt + tile * 64, 1u, __ATOMIC_RELAXED, __HIP_MEMORY_SCOPE_AGENT);
            for (int spin = 0; spin < (1 << 22); ++spin) {
                if (__hip_atomic_load(cnt + tile * 64, __ATOMIC_RELAXED, __HIP_MEMORY_SCOPE_AGENT) >= 3u) break;
                __builtin_amdgcn_s_sleep(1);
            }
        }
        asm volatile("s_waitcnt vmcnt(0) lgkmcnt(0)\n\ts_barrier" ::: "memory");
        constexpr int SA_ = (C == 0) ? 1 : 0, SB_ = (C == 2) ? 1 : 2;
        f32x4 pa[4], pb[4], pg[2];
#pragma unroll
        for (int u = 0; u < NU; ++u) { const int i = U0 + u;
            ld16_sc1<0>(pa[u], avo, pbase + (size_t)SA_ * 65536 + i * 1024); ld16_sc1<0>(pb[u], avo, pbase + (size_t)SB_ * 65536 + i * 1024);
            if constexpr (C != 2) ld16_sc1<0>(pg[u], avo, pbase + (size_t)3 * 65536 + i * 1024); }
        if constexpr (C != 2) asm volatile("s_waitcnt vmcnt(0)" : "+v"(pa[0]), "+v"(pa[1]), "+v"(pb[0]), "+v"(pb[1]), "+v"(pg[0]), "+v"(pg[1]) :: "memory");
        else                  asm volatile("s_waitcnt vmcnt(0)" : "+v"(pa[0]), "+v"(pa[1]), "+v"(pa[2]), "+v"(pa[3]), "+v"(pb[0]), "+v"(pb[1]), "+v"(pb[2]), "+v"(pb[3]) :: "memory");
#pragma unroll
        for (int u = 0; u < NU; ++u) { const int i = U0 + u;
            float r[4];
#pragma unroll
            for (int j = 0; j < 4; ++j) {
                const float xv = (j & 1) ? __uint_as_float(xb[u][j >> 1] & 0xffff0000u) : __uint_as_float(xb[u][j >> 1] << 16);
                const float bv = (j & 1) ? __uint_as_float(bb[u][j >> 1] & 0xffff0000u) : __uint_as_float(bb[u][j >> 1] << 16);
                const float p0 = (C == 0) ? ra[i][j] : pa[u][j];
                const float p1 = (C == 0) ? pa[u][j] : (C == 1) ? ra[i][j] : pb[u][j];
                const float p2 = (C == 2) ? ra[i][j] : pb[u][j];
                const float gv = (C == 2) ? rb[i][j] : pg[u][j];
                const float s = rbf((p0 + p1) + p2);
                const float mm = rbf(rbf(gv) + bv);
                const float gt = rbf(mm * __builtin_amdgcn_rcpf(1.f + __builtin_amdgcn_exp2f(-mm * 1.4426950408889634f)));
                r[j] = xv + rbf(s * gt);
            }
            u32x2 w; w[0] = cvtpk(r[0], r[1]); w[1] = cvtpk(r[2], r[3]);
            *(u32x2*)(out + (size_t)row * DM + col0 + 32 * (i >> 2) + 8 * (i & 3)) = w;
        }
    };
    if (chunk == 0) finish(std::integral_constant<int, 0>{}); else if (chunk == 1) finish(std::integral_constant<int, 1>{}); else finish(std::integral_constant<int, 2>{});
}

extern "C" void kernel_launch(void* const* d_in, const int* in_sizes, int n_in, void* d_out, int out_size, void* d_ws, size_t ws_size, hipStream_t stream) {
    const u16* x  = (const u16*)d_in[0];
    const u16* kc = (const u16*)d_in[1];
    const u16* vc = (const u16*)d_in[2];
    const u16* wq = (const u16*)d_in[3];
    const u16* wo = (const u16*)d_in[4];
    const u16* w1 = (const u16*)d_in[6];
    const u16* b1 = (const u16*)d_in[7];
    const u16* w2 = (const u16*)d_in[8];
    const u16* b2 = (const u16*)d_in[9];
    char* ws = (char*)d_ws;
    u16* xn = (u16*)ws;
    float* st2 = (float*)(ws + (size_t)4 * 1024 * 1024);
    float* w1s = (float*)(ws + (size_t)5 * 1024 * 1024);
    u16* hb = (u16*)(ws + (size_t)8 * 1024 * 1024);
    u16* ob = (u16*)(ws + (size_t)16 * 1024 * 1024);
    u16* wpk = (u16*)(ws + (size_t)96 * 1024 * 1024);
    unsigned* cnt = (unsigned*)(ws + (size_t)6 * 1024 * 1024);
    float* part = (float*)(ws + (size_t)48 * 1024 * 1024);
    hipFuncSetAttribute((const void*)k1_attn, hipFuncAttributeMaxDynamicSharedMemorySize, K1_LDS);
    hipFuncSetAttribute((const void*)k2_gemm, hipFuncAttributeMaxDynamicSharedMemorySize, K2_LDS);
    hipLaunchKernelGGL(k0_ln, dim3(K0_LN + K0_WS), dim3(256), 0, stream, x, w1, xn, st2, w1s, cnt);
    hipLaunchKernelGGL(k1_attn, dim3(256), dim3(512), K1_LDS, stream, xn, st2, w1s, kc, vc, wq, w1, b1, ob, hb, wo, w2, wpk
        );
    hipLaunchKernelGGL(k2_gemm, dim3(240), dim3(512), K2_LDS, stream, ob, hb, wpk, part, cnt, x, b2, (u16*)d_out
        );
}
```

```cpp
#include <hip/hip_runtime.h>
#include <hip/hip_bf16.h>
#include <stdint.h>
#include <type_traits>

using bf16x8 = __attribute__((ext_vector_type(8))) short;
using s16x4  = __attribute__((ext_vector_type(4))) short;
using f32x16 = __attribute__((ext_vector_type(16))) float;
using f32x4  = __attribute__((ext_vector_type(4))) float;
using u32x4  = __attribute__((ext_vector_type(4))) unsigned;
using u32x2  = __attribute__((ext_vector_type(2))) unsigned;
typedef short v4i16_t __attribute__((ext_vector_type(4)));
typedef __attribute__((address_space(3))) const char* lds_cptr;
typedef unsigned short u16;

constexpr int SEQ = 2048, DM = 640, NH = 32, DH = 192, DQ = 6144, FF = 1536;

#define DI __device__ __forceinline__
#define SBAR() __builtin_amdgcn_sched_barrier(0)
#define PIN(x) asm volatile("" : "+v"(x))
#define MFMA(a, b, c) __builtin_amdgcn_mfma_f32_32x32x16_bf16(a, b, c, 0, 0, 0)
#define WAIT_BAR(N) asm volatile("s_waitcnt vmcnt(" #N ") lgkmcnt(0)\n\ts_barrier" ::: "memory")

typedef float f32x2 __attribute__((ext_vector_type(2)));
typedef __bf16 bf16x2_t __attribute__((ext_vector_type(2)));
DI unsigned cvtpk(float lo, float hi) { f32x2 v = {lo, hi}; bf16x2_t b = __builtin_convertvector(v, bf16x2_t); return __builtin_bit_cast(unsigned, b); }
DI float rbf(float x) { return __uint_as_float(cvtpk(x, x) << 16); }
DI void gldss(unsigned voff, const void* sbase, unsigned lds_base) {
    asm volatile("s_mov_b32 m0, %2\n\ts_nop 0\n\tglobal_load_lds_dwordx4 %0, %1" :: "v"(voff), "s"(sbase), "s"(lds_base) : "memory");
}
DI bf16x8 ldsr(lds_cptr p) { return *(const __attribute__((address_space(3))) bf16x8*)p; }
DI s16x4 vtr(lds_cptr p) { return __builtin_bit_cast(s16x4, __builtin_amdgcn_ds_read_tr16_b64_v4i16((__attribute__((address_space(3))) v4i16_t*)p)); }
DI bf16x8 cat(s16x4 a, s16x4 b) { return __builtin_shufflevector(a, b, 0, 1, 2, 3, 4, 5, 6, 7); }
DI unsigned rfl(unsigned x) { return (unsigned)__builtin_amdgcn_readfirstlane((int)x); }
DI float max3a(float a, float b, float c) { float r; asm("v_max3_f32 %0, %1, %2, %3" : "=v"(r) : "v"(a), "v"(b), "v"(c)); return r; }
DI float xhalf_max(float m) { auto rr = __builtin_amdgcn_permlane32_swap(__float_as_uint(m), __float_as_uint(m), false, false); return __builtin_fmaxf(__uint_as_float(rr[0]), __uint_as_float(rr[1])); }
DI float xhalf_sum(float m) { auto rr = __builtin_amdgcn_permlane32_swap(__float_as_uint(m), __float_as_uint(m), false, false); return __uint_as_float(rr[0]) + __uint_as_float(rr[1]); }

template <int CTRL, int RM> DI float dpp_add(float v) { return v + __uint_as_float((unsigned)__builtin_amdgcn_update_dpp(0, (int)__float_as_uint(v), CTRL, RM, 0xf, true)); }
DI float wave_sum(float v) {
    v = dpp_add<0x111, 0xf>(v); v = dpp_add<0x112, 0xf>(v); v = dpp_add<0x114, 0xf>(v); v = dpp_add<0x118, 0xf>(v);
    v = dpp_add<0x142, 0xa>(v); v = dpp_add<0x143, 0xc>(v);
    return __uint_as_float((unsigned)__builtin_amdgcn_readlane((int)__float_as_uint(v), 63));
}
constexpr int K0_LN = SEQ / 4, K0_WS = FF / 64;
__global__ __launch_bounds__(256) void k0_ln(const u16* __restrict__ x, const u16* __restrict__ w1, u16* __restrict__ xn, float* __restrict__ st2, float* __restrict__ w1s, unsigned* __restrict__ cnt) {
    const int lane = threadIdx.x & 63;
    if (blockIdx.x == 0 && threadIdx.x < 160) cnt[threadIdx.x * 32] = 0u;
    if (blockIdx.x >= SEQ / 4) {
        __shared__ float red[32][65];
        const int cg = threadIdx.x & 7, rg = threadIdx.x >> 3, col0 = (blockIdx.x - SEQ / 4) * 64;
        const u32x4* src = (const u32x4*)(w1 + (size_t)(rg * 20) * FF + col0 + cg * 8);
        u32x4 t[20];
#pragma unroll
        for (int i = 0; i < 20; ++i) t[i] = src[(size_t)i * (FF / 8)];
        float a[8] = {0.f, 0.f, 0.f, 0.f, 0.f, 0.f, 0.f, 0.f};
#pragma unroll
        for (int i = 0; i < 20; ++i)
#pragma unroll
            for (int e = 0; e < 4; ++e) { a[2 * e] += __uint_as_float(t[i][e] << 16); a[2 * e + 1] += __uint_as_float(t[i][e] & 0xffff0000u); }
#pragma unroll
        for (int e = 0; e < 8; ++e) red[rg][cg * 8 + e] = a[e];
        __syncthreads();
        if (threadIdx.x < 64) { float s = 0.f;
#pragma unroll
            for (int r = 0; r < 32; ++r) s += red[r][threadIdx.x];
            w1s[col0 + threadIdx.x] = s; }
        return;
    }
    const int row = blockIdx.x * 4 + (threadIdx.x >> 6);
    const u32x4* xr = (const u32x4*)(x + (size_t)row * DM);
    const bool two = lane < 16;
    u32x4 w0 = xr[lane], w1v = two ? xr[64 + lane] : (u32x4){0u, 0u, 0u, 0u};
    float v[16];
#pragma unroll
    for (int i = 0; i < 4; ++i) { v[2 * i] = __uint_as_float(w0[i] << 16); v[2 * i + 1] = __uint_as_float(w0[i] & 0xffff0000u);
                                  v[8 + 2 * i] = __uint_as_float(w1v[i] << 16); v[8 + 2 * i + 1] = __uint_as_float(w1v[i] & 0xffff0000u); }
    const size_t base = (size_t)(row >> 5) * 40 * 64 * 8 + (size_t)(row & 31) * 8;
    const size_t off0 = base + (size_t)(lane >> 1) * 512 + (lane & 1) * 256;
    const size_t off1 = base + (size_t)((64 + lane) >> 1) * 512 + (lane & 1) * 256;
#pragma unroll
    for (int pass = 0; pass < 2; ++pass) {
        float s = 0.f;
#pragma unroll
        for (int i = 0; i < 16; ++i) s += v[i];
        const float mean = wave_sum(s) * (1.0f / DM);
        float q = 0.f;
#pragma unroll
        for (int i = 0; i < 8; ++i) { float d = v[i] - mean; q += d * d; }
        if (two) {
#pragma unroll
            for (int i = 8; i < 16; ++i) { float d = v[i] - mean; q += d * d; }
        }
        const float var = wave_sum(q) * (1.0f / DM);
        const float mb = rbf(mean), vb = rbf(var);
        const float den = rbf(__fsqrt_rn(rbf(vb + 1e-5f)));
        if (pass == 1) { if (lane == 0) { st2[2 * row] = mb; st2[2 * row + 1] = den; } break; }
#pragma unroll
        for (int i = 0; i < 16; ++i) v[i] = rbf(__fdiv_rn(rbf(v[i] - mb), den));
        if (!two) {
#pragma unroll
            for (int i = 8; i < 16; ++i) v[i] = 0.f;
        }
        u32x4 a, b;
#pragma unroll
        for (int i = 0; i < 4; ++i) { a[i] = (__float_as_uint(v[2 * i]) >> 16) | (__float_as_uint(v[2 * i + 1]) & 0xffff0000u);
                                      b[i] = (__float_as_uint(v[8 + 2 * i]) >> 16) | (__float_as_uint(v[8 + 2 * i + 1]) & 0xffff0000u); }
        *(u32x4*)(xn + off0) = a;
        if (two) *(u32x4*)(xn + off1) = b;
    }
}

constexpr int SLOT = 24576, LDS_K = 0, LDS_V = 3 * SLOT, PSLOT = 32768, K1_LDS = 163840;
constexpr float CSC = 0.07216878364870322f * 1.4426950408889634f;
constexpr float THR = 8.f;

DI float gelu_tanh(float x) {
    const float e = __builtin_amdgcn_exp2f(x * __builtin_fmaf(x * x, 0.10294324f, 2.3022082f));
    return __builtin_fmaf(-x, __builtin_amdgcn_rcpf(e + 1.f), x);
}
template <int OFF> DI void gld16(bf16x8& d, unsigned voff, const void* sb) { asm volatile("global_load_dwordx4 %0, %1, %2 offset:%3" : "=v"(d) : "v"(voff), "s"(sb), "i"(OFF) : "memory"); }
template <int OFF> DI void gld16f(f32x4& d, unsigned voff, const void* sb) { asm volatile("global_load_dwordx4 %0, %1, %2 offset:%3" : "=v"(d) : "v"(voff), "s"(sb), "i"(OFF) : "memory"); }
template <int OFF> DI void gld8(u32x2& d, unsigned voff, const void* sb) { asm volatile("global_load_dwordx2 %0, %1, %2 offset:%3" : "=v"(d) : "v"(voff), "s"(sb), "i"(OFF) : "memory"); }

template <int I> DI bf16x8 pfrag(lds_cptr wqp, lds_cptr w1p) {
    constexpr int s = I >> 3, j = I & 7;
    if constexpr (j < 6) { constexpr int off = 2 * s * 3072 + (j >> 1) * 1024 + (j & 1) * 512; return cat(vtr(wqp + off), vtr(wqp + off + 256)); }
    else                 { constexpr int off = 2 * s * 1024 + (j - 6) * 512; return cat(vtr(w1p + off), vtr(w1p + off + 256)); }
}
template <int I, bool NEXT, class H> DI void pstep(f32x16 (&o6)[6], f32x16 (&hacc)[2], bf16x8 (&f)[4], const bf16x8 (&xf)[4], lds_cptr wqp, lds_cptr w1p, lds_cptr wqn, lds_cptr w1n, H&& hook) {
    constexpr int s = I >> 3, j = I & 7;
    if constexpr (I + 3 < 32) f[(I + 3) & 3] = pfrag<I + 3>(wqp, w1p);
    else if constexpr (NEXT) f[(I + 3) & 3] = pfrag<I + 3 - 32>(wqn, w1n);
    if constexpr (j < 6) { o6[j] = MFMA(f[I & 3], xf[s], o6[j]); PIN(o6[j]); } else { hacc[j - 6] = MFMA(f[I & 3], xf[s], hacc[j - 6]); PIN(hacc[j - 6]); }
    hook(std::integral_constant<int, I>{});
    asm volatile("" ::: "memory"); SBAR();
    if constexpr (I + 1 < 32) pstep<I + 1, NEXT>(o6, hacc, f, xf, wqp, w1p, wqn, w1n, hook);
}

__global__ __launch_bounds__(512, 2)
void k1_attn(const u16* __restrict__ xn, const float* __restrict__ st2, const float* __restrict__ w1sum, const u16* __restrict__ Kc, const u16* __restrict__ Vc,
             const u16* __restrict__ wq, const u16* __restrict__ w1, const u16* __restrict__ b1,
             u16* __restrict__ o, u16* __restrict__ hbuf, const u16* __restrict__ wo, const u16* __restrict__ w2, u16* __restrict__ wpk
             ) {
    extern __shared__ __attribute__((aligned(16))) char lds[];
    const int tid = threadIdx.x, lane = tid & 63, r32 = lane & 31, hi = lane >> 5;
    const int wid = __builtin_amdgcn_readfirstlane(tid >> 6);
    const int bid = blockIdx.x, xcd = bid & 7, jj = bid >> 3;
    const int head = xcd + 8 * (jj >> 3), qb = jj & 7;
    const int row0 = qb * 256 + wid * 32;
    const unsigned lds0 = (unsigned)(uintptr_t)lds;
    const lds_cptr L = (lds_cptr)lds;

    const int vd1 = lane >> 5, vr7 = (lane >> 2) & 7, vch = lane & 3;
    const int kr7 = ((lane >> 4) << 1) | (((lane >> 3) & 1) ^ ((wid >> 1) & 1)), kc8 = (lane & 7) ^ kr7;
    const int q4 = (lane & 15) >> 2, p4 = lane & 3, g1 = (lane >> 4) & 1;
    const int r7 = r32 & 7;

    const char* kbase = (const char*)(Kc + (size_t)head * SEQ * DH);
    const char* vbase = (const char*)(Vc + (size_t)head * SEQ * DH);
    const unsigned kvo = kr7 * (DH * 2) + kc8 * 16, vvo = vr7 * (DH * 2) + vd1 * 64 + vch * 16;
    const unsigned kdst = lds0 + LDS_K + wid * 3072, vdst = lds0 + LDS_V + wid * 3072;

    f32x16 o6[6];
    f32x16 hacc[2];
#pragma unroll
    for (int i = 0; i < 6; ++i) o6[i] = f32x16{};
    hacc[0] = f32x16{}; hacc[1] = f32x16{};
    const int fcol0 = (48 * head < FF - 64) ? 48 * head : FF - 64;
    u32x2 bb[8], stv; f32x4 wsv[8];
    {
        const char* wqs = (const char*)(wq + (size_t)(8 * wid) * DQ + head * DH);
        const char* w1s = (const char*)(w1 + (size_t)(8 * wid) * FF + fcol0);
        const unsigned wqo = vr7 * (DQ * 2) + vd1 * 64 + vch * 16, w1o = vr7 * (FF * 2) + vd1 * 64 + vch * 16;
        const unsigned wqd = lds0 + wid * 3072, w1d = lds0 + 24576 + wid * 1024;
#define CSRC(c) (c)
#define DMA_W(c) do { \
            gldss(wqo, wqs + (size_t)(c) * 64 * DQ * 2,       wqd + ((c) % 5) * PSLOT); \
            gldss(wqo, wqs + (size_t)(c) * 64 * DQ * 2 + 128, wqd + ((c) % 5) * PSLOT + 1024); \
            gldss(wqo, wqs + (size_t)(c) * 64 * DQ * 2 + 256, wqd + ((c) % 5) * PSLOT + 2048); \
            gldss(w1o, w1s + (size_t)(c) * 64 * FF * 2,       w1d + ((c) % 5) * PSLOT); } while (0)
        const int p4s = ((p4 & 1) << 1) | (p4 >> 1);
        const lds_cptr wqp = L + hi * 3072 + q4 * 64 + g1 * 32 + p4s * 8;
        const lds_cptr w1p = L + 24576 + hi * 1024 + q4 * 64 + g1 * 32 + p4 * 8;
        const char* xsrc = (const char*)(xn + (size_t)(row0 >> 5) * 40 * 512);
        const char* bsrc = (const char*)(b1 + fcol0);
        const char* wssrc = (const char*)(w1sum + fcol0);
        const char* stsrc = (const char*)(st2 + 2 * row0);
        const unsigned xo = lane * 16, bo = 8 * hi, wso = 16 * hi, sto = 8 * r32;
        bf16x8 xa[4], xb[4], f[4];
#define LDX(c, X) do { const char* xp_ = xsrc + CSRC(c) * 4096; \
            gld16<0>(X[0], xo, xp_); gld16<1024>(X[1], xo, xp_); gld16<2048>(X[2], xo, xp_); gld16<3072>(X[3], xo, xp_); } while (0)
#define WAITX(N, X) asm volatile("s_waitcnt vmcnt(" #N ") lgkmcnt(0)\n\ts_barrier" : "+v"(X[0]), "+v"(X[1]), "+v"(X[2]), "+v"(X[3]) :: "memory")
#define PCOMP(c, X, PRE, NEXT, HOOK) do { lds_cptr a_ = wqp + ((c) % 5) * PSLOT; lds_cptr b_ = w1p + ((c) % 5) * PSLOT; lds_cptr an_ = wqp + (((c) + 1) % 5) * PSLOT; lds_cptr bn_ = w1p + (((c) + 1) % 5) * PSLOT; \
            PIN(a_); PIN(b_); PIN(an_); PIN(bn_); \
            if (!(PRE)) { f[0] = pfrag<0>(a_, b_); f[1] = pfrag<1>(a_, b_); f[2] = pfrag<2>(a_, b_); } SBAR(); \
            pstep<0, NEXT>(o6, hacc, f, X, a_, b_, an_, bn_, HOOK); } while (0)
#define XHOOK(cn, XN) if constexpr (I == 1) gld16<0>(XN[0], xo, xsrc + (cn) * 4096); if constexpr (I == 4) gld16<1024>(XN[1], xo, xsrc + (cn) * 4096); \
                      if constexpr (I == 7) gld16<2048>(XN[2], xo, xsrc + (cn) * 4096); if constexpr (I == 10) gld16<3072>(XN[3], xo, xsrc + (cn) * 4096);
#define WHOOK(cw) if constexpr (I == 13) gldss(wqo, wqs + (size_t)(cw) * 64 * DQ * 2,       wqd + ((cw) % 5) * PSLOT); \
                  if constexpr (I == 17) gldss(wqo, wqs + (size_t)(cw) * 64 * DQ * 2 + 128, wqd + ((cw) % 5) * PSLOT + 1024); \
                  if constexpr (I == 21) gldss(wqo, wqs + (size_t)(cw) * 64 * DQ * 2 + 256, wqd + ((cw) % 5) * PSLOT + 2048); \
                  if constexpr (I == 25) gldss(w1o, w1s + (size_t)(cw) * 64 * FF * 2,       w1d + ((cw) % 5) * PSLOT);
#define KHOOK(t, slot, I0) { const char* s_ = kbase + (size_t)((64 * (t) + 32 + 8 * wid) & (SEQ - 1)) * (DH * 2); \
                  if constexpr (I == (I0)) gldss(kvo, s_, kdst + (slot)); if constexpr (I == (I0) + 3) gldss(kvo, s_ + 128, kdst + (slot) + 1024); if constexpr (I == (I0) + 6) gldss(kvo, s_ + 256, kdst + (slot) + 2048); }
#define VHOOK(t, slot, I0) { const char* s_ = vbase + (size_t)((64 * (t) + 8 * wid) & (SEQ - 1)) * (DH * 2); \
                  if constexpr (I == (I0)) gldss(vvo, s_, vdst + (slot)); if constexpr (I == (I0) + 3) gldss(vvo, s_ + 128, vdst + (slot) + 1024); if constexpr (I == (I0) + 6) gldss(vvo, s_ + 256, vdst + (slot) + 2048); }
#define HK(body) [&](auto Ic) { constexpr int I = decltype(Ic)::value; body }
        LDX(0, xa); DMA_W(0); DMA_W(1); DMA_W(2); DMA_W(3);
        WAITX(12, xa); PCOMP(0, xa, false, false, HK(XHOOK(1, xb) WHOOK(4)));
        WAITX(4, xb);  PCOMP(1, xb, false, true,  HK(XHOOK(2, xa) WHOOK(5)));
        WAITX(4, xa);  PCOMP(2, xa, true,  true,  HK(XHOOK(3, xb) WHOOK(6)));
        WAITX(4, xb);  PCOMP(3, xb, true,  true,  HK(XHOOK(4, xa) WHOOK(7)));
        WAITX(4, xa);  PCOMP(4, xa, true,  true,  HK(XHOOK(5, xb) WHOOK(8)));
        WAITX(4, xb);  PCOMP(5, xb, true,  true,  HK(XHOOK(6, xa) WHOOK(9)));
        WAITX(4, xa);  PCOMP(6, xa, true,  true,  HK(XHOOK(7, xb) KHOOK(-1, 0, 13)));
        WAITX(3, xb);  PCOMP(7, xb, true,  true,  HK(XHOOK(8, xa) KHOOK(0, SLOT, 13)));
        WAITX(3, xa);  PCOMP(8, xa, true,  true,  HK(XHOOK(9, xb) KHOOK(1, 2 * SLOT, 13) VHOOK(0, 0, 22)));
        WAITX(6, xb);  PCOMP(9, xb, true,  false, HK(
            if constexpr (I == 1) { gld8<0>(bb[0], bo, bsrc); gld8<16>(bb[1], bo, bsrc); } if constexpr (I == 2) { gld8<32>(bb[2], bo, bsrc); gld8<48>(bb[3], bo, bsrc); }
            if constexpr (I == 3) { gld8<64>(bb[4], bo, bsrc); gld8<80>(bb[5], bo, bsrc); } if constexpr (I == 4) { gld8<96>(bb[6], bo, bsrc); gld8<112>(bb[7], bo, bsrc); }
            if constexpr (I == 5) { gld16f<0>(wsv[0], wso, wssrc); gld16f<32>(wsv[1], wso, wssrc); } if constexpr (I == 6) { gld16f<64>(wsv[2], wso, wssrc); gld16f<96>(wsv[3], wso, wssrc); }
            if constexpr (I == 7) { gld16f<128>(wsv[4], wso, wssrc); gld16f<160>(wsv[5], wso, wssrc); } if constexpr (I == 8) { gld16f<192>(wsv[6], wso, wssrc); gld16f<224>(wsv[7], wso, wssrc); }
            if constexpr (I == 9) gld8<0>(stv, sto, stsrc);
            VHOOK(1, SLOT, 13)));
        asm volatile("s_waitcnt vmcnt(3)" : "+v"(bb[0]), "+v"(bb[1]), "+v"(bb[2]), "+v"(bb[3]), "+v"(bb[4]), "+v"(bb[5]), "+v"(bb[6]), "+v"(bb[7]),
                     "+v"(wsv[0]), "+v"(wsv[1]), "+v"(wsv[2]), "+v"(wsv[3]), "+v"(wsv[4]), "+v"(wsv[5]), "+v"(wsv[6]), "+v"(wsv[7]), "+v"(stv) :: "memory");
#undef DMA_W
#undef LDX
#undef WAITX
#undef PCOMP
    }
    bf16x8 qf[12];
#pragma unroll
    for (int db = 0; db < 6; ++db)
#pragma unroll
        for (int s = 0; s < 2; ++s) {
            u32x4 w;
#pragma unroll
            for (int e = 0; e < 4; ++e) w[e] = cvtpk(o6[db][8 * s + 2 * e] * CSC, o6[db][8 * s + 2 * e + 1] * CSC);
            qf[2 * db + s] = __builtin_bit_cast(bf16x8, w);
        }
    {
        const int flo = 48 * head - fcol0;
        const float m2 = __uint_as_float(stv[0]), rden = __fdiv_rn(1.0f, __uint_as_float(stv[1]));
        char* hbase = (char*)hbuf + (size_t)(row0 >> 5) * 96 * 1024 + r32 * 16 + 8 * hi;
#pragma unroll
        for (int ft = 0; ft < 2; ++ft)
#pragma unroll
            for (int g = 0; g < 4; ++g) {
                const int fl = 32 * ft + 8 * g;
                if (fl >= flo && fl < flo + 48) {
                    float hv[4];
#pragma unroll
                    for (int e = 0; e < 4; ++e) {
                        const unsigned bw = bb[4 * ft + g][e >> 1];
                        const float bias = (e & 1) ? __uint_as_float(bw & 0xffff0000u) : __uint_as_float(bw << 16);
                        hv[e] = gelu_tanh(rbf(rbf(__builtin_fmaf(-m2, wsv[4 * ft + g][e], hacc[ft][4 * g + e]) * rden) + bias));
                    }
                    u32x2 w; w[0] = cvtpk(hv[0], hv[1]); w[1] = cvtpk(hv[2], hv[3]);
                    const int fc = (fcol0 + fl) >> 3;
                    *(u32x2*)(hbase + (fc >> 1) * 1024 + (fc & 1) * 512) = w;
                }
            }
    }
#pragma unroll
    for (int i = 0; i < 6; ++i) o6[i] = f32x16{};

    lds_cptr kp[4];
#pragma unroll
    for (int j = 0; j < 4; ++j)
        kp[j] = L + LDS_K + (r32 >> 3) * 3072 + ((r7 >> 1) << 8) + ((((r32 >> 4) & 1) ^ (r7 & 1)) << 7) + ((r7 ^ (2 * j + hi)) << 4);
    const lds_cptr vp = L + LDS_V + (4 * hi + q4) * 64 + g1 * 32 + p4 * 8;

    f32x16 SA, SB, NEGM; float mref, lsum = 0.f;
    u32x4 P0 = {0u,0u,0u,0u}, P1 = {0u,0u,0u,0u};
#define KRD(sl_, half, ks) ldsr(kp[(ks) & 3] + (sl_) + (half) * 12288 + ((ks) >> 2) * 1024)
#define VRD(sl_, half, s, db) cat(vtr(vp + (sl_) + (4 * (half) + 2 * (s)) * 3072 + ((db) >> 1) * 1024 + ((db) & 1) * 512), \
                                  vtr(vp + (sl_) + (4 * (half) + 2 * (s) + 1) * 3072 + ((db) >> 1) * 1024 + ((db) & 1) * 512))
    {
        SA = f32x16{};
#pragma unroll
        for (int ks = 0; ks < 12; ++ks) SA = MFMA(KRD(0, 1, ks), qf[ks], SA);
        float mx = SA[0];
#pragma unroll
        for (int i = 1; i < 16; ++i) mx = __builtin_fmaxf(mx, SA[i]);
        mref = xhalf_max(mx);
#pragma unroll
        for (int i = 0; i < 16; ++i) { SA[i] -= mref; NEGM[i] = -mref; }
    }
    WAIT_BAR(3);

#define EX(v) __builtin_amdgcn_exp2f(v)
#define MX3(a, b, c) max3a((a), (b), (c))
#define GEND() do { asm volatile("" ::: "memory"); SBAR(); } while (0)
#define HSTEP(SC, SN, ksl, khalf, vsl, vhalf, nksl, nkhalf, D0, D1, D2, D3, D4, D5) do { \
        float sa = 0.f, sb = 0.f, mx = 0.f; \
        k3 = KRD(ksl, khalf, 3);  SN = MFMA(k0, qf[0], NEGM); PIN(SN); float p0 = EX(SC[0]); PIN(p0); GEND(); \
        k0 = KRD(ksl, khalf, 4);  SN = MFMA(k1, qf[1], SN); PIN(SN); D0; float p1 = EX(SC[1]); PIN(p1); GEND(); \
        k1 = KRD(ksl, khalf, 5);  SN = MFMA(k2, qf[2], SN); PIN(SN); float p2 = EX(SC[2]); P0[0] = cvtpk(p0, p1); PIN(p2); PIN(P0); GEND(); \
        k2 = KRD(ksl, khalf, 6);  SN = MFMA(k3, qf[3], SN); PIN(SN); float p3 = EX(SC[3]); sa = p0 + p1; PIN(p3); PIN(sa); GEND(); \
        k3 = KRD(ksl, khalf, 7);  SN = MFMA(k0, qf[4], SN); PIN(SN); float p4 = EX(SC[4]); P0[1] = cvtpk(p2, p3); PIN(p4); PIN(P0); GEND(); \
        k0 = KRD(ksl, khalf, 8);  SN = MFMA(k1, qf[5], SN); PIN(SN); D1; float p5 = EX(SC[5]); sb = p2 + p3; PIN(p5); PIN(sb); GEND(); \
        k1 = KRD(ksl, khalf, 9);  SN = MFMA(k2, qf[6], SN); PIN(SN); float p6 = EX(SC[6]); P0[2] = cvtpk(p4, p5); PIN(p6); PIN(P0); GEND(); \
        k2 = KRD(ksl, khalf, 10); SN = MFMA(k3, qf[7], SN); PIN(SN); float p7 = EX(SC[7]); sa += p4; sb += p5; PIN(p7); PIN(sa); PIN(sb); GEND(); \
        k3 = KRD(ksl, khalf, 11); SN = MFMA(k0, qf[8], SN); PIN(SN); float p8 = EX(SC[8]); P0[3] = cvtpk(p6, p7); PIN(p8); PIN(P0); GEND(); \
        bf16x8 v0 = VRD(vsl, vhalf, 0, 0); SN = MFMA(k1, qf[9], SN); PIN(SN); D2; float p9 = EX(SC[9]); sa += p6; sb += p7; PIN(p9); PIN(sa); PIN(sb); GEND(); \
        bf16x8 v1 = VRD(vsl, vhalf, 0, 1); SN = MFMA(k2, qf[10], SN); PIN(SN); float p10 = EX(SC[10]); P1[0] = cvtpk(p8, p9); PIN(p10); PIN(P1); GEND(); \
        bf16x8 v2 = VRD(vsl, vhalf, 0, 2); SN = MFMA(k3, qf[11], SN); PIN(SN); float p11 = EX(SC[11]); sa += p8; sb += p9; PIN(p11); PIN(sa); PIN(sb); GEND(); \
        const bf16x8 pa0 = __builtin_bit_cast(bf16x8, P0); \
        bf16x8 v3; \
        v3 = VRD(vsl, vhalf, 0, 3); o6[0] = MFMA(v0, pa0, o6[0]); PIN(o6[0]); float p12 = EX(SC[12]); P1[1] = cvtpk(p10, p11); PIN(p12); PIN(P1); GEND(); \
        v0 = VRD(vsl, vhalf, 0, 4); o6[1] = MFMA(v1, pa0, o6[1]); PIN(o6[1]); D3; float p13 = EX(SC[13]); sa += p10; sb += p11; PIN(p13); PIN(sa); PIN(sb); GEND(); \
        v1 = VRD(vsl, vhalf, 0, 5); o6[2] = MFMA(v2, pa0, o6[2]); PIN(o6[2]); float p14 = EX(SC[14]); P1[2] = cvtpk(p12, p13); PIN(p14); PIN(P1); GEND(); \
        v2 = VRD(vsl, vhalf, 1, 0); o6[3] = MFMA(v3, pa0, o6[3]); PIN(o6[3]); float p15 = EX(SC[15]); sa += p12; sb += p13; PIN(p15); PIN(sa); PIN(sb); GEND(); \
        v3 = VRD(vsl, vhalf, 1, 1); o6[4] = MFMA(v0, pa0, o6[4]); PIN(o6[4]); P1[3] = cvtpk(p14, p15); mx = MX3(SN[0], SN[1], SN[2]); mx = MX3(mx, SN[3], SN[4]); PIN(P1); PIN(mx); GEND(); \
        v0 = VRD(vsl, vhalf, 1, 2); o6[5] = MFMA(v1, pa0, o6[5]); PIN(o6[5]); D4; sa += p14; sb += p15; mx = MX3(mx, SN[5], SN[6]); PIN(sa); PIN(sb); PIN(mx); GEND(); \
        const bf16x8 pa1 = __builtin_bit_cast(bf16x8, P1); \
        v1 = VRD(vsl, vhalf, 1, 3); o6[0] = MFMA(v2, pa1, o6[0]); PIN(o6[0]); mx = MX3(mx, SN[7], SN[8]); lsum += sa + sb; PIN(mx); PIN(lsum); GEND(); \
        v2 = VRD(vsl, vhalf, 1, 4); o6[1] = MFMA(v3, pa1, o6[1]); PIN(o6[1]); mx = MX3(mx, SN[9], SN[10]); mx = MX3(mx, SN[11], SN[12]); PIN(mx); GEND(); \
        v3 = VRD(vsl, vhalf, 1, 5); o6[2] = MFMA(v0, pa1, o6[2]); PIN(o6[2]); mx = MX3(mx, SN[13], SN[14]); mx = max3a(mx, SN[15], SN[15]); PIN(mx); GEND(); \
        k0 = KRD(nksl, nkhalf, 0);  o6[3] = MFMA(v1, pa1, o6[3]); PIN(o6[3]); D5; GEND(); \
        k1 = KRD(nksl, nkhalf, 1);  o6[4] = MFMA(v2, pa1, o6[4]); PIN(o6[4]); GEND(); \
        k2 = KRD(nksl, nkhalf, 2);  o6[5] = MFMA(v3, pa1, o6[5]); PIN(o6[5]); GEND(); \
        if (__builtin_expect(__any(mx > THR), 0)) { \
            const float dl = __builtin_fmaxf(xhalf_max(mx), 0.f); mref += dl; const float f = __builtin_amdgcn_exp2f(-dl); lsum *= f; \
            _Pragma("unroll") for (int r_ = 0; r_ < 16; ++r_) { SN[r_] -= dl; NEGM[r_] = -mref; } \
            _Pragma("unroll") for (int d_ = 0; d_ < 6; ++d_) _Pragma("unroll") for (int r_ = 0; r_ < 16; ++r_) o6[d_][r_] *= f; \
        } \
    } while (0)

    bf16x8 k0 = KRD(SLOT, 0, 0), k1 = KRD(SLOT, 0, 1), k2 = KRD(SLOT, 0, 2), k3;
    u32x4 pkv = {0u, 0u, 0u, 0u};
    const int pk0 = (bid * 8 + wid) * 5;
    const unsigned pko = vr7 * (DM * 2) + vd1 * 64 + vch * 16, xo16 = lane * 16;
    int s0 = 0;
    auto pk_store = [&](int t) { const int P = (pk0 + t - 1 < 9600) ? pk0 + t - 1 : 9599;
        if (t >= 1) asm volatile("s_waitcnt vmcnt(6)\n\tglobal_store_dwordx4 %0, %1, %2\n\ts_nop 1" :: "v"(xo16), "v"(pkv), "s"((const char*)wpk + (size_t)P * 1024) : "memory"); };
    auto pk_load = [&](int t) { const int P = (pk0 + t < 9600) ? pk0 + t : 9599, b = P >> 4, tn = b / 120, g = b - tn * 120;
        const char* src = ((g < 96) ? (const char*)(wo + (size_t)g * 64 * DM) : (const char*)(w2 + (size_t)(g - 96) * 64 * DM)) + (size_t)(8 * ((P >> 1) & 7)) * (DM * 2) + tn * 256 + (P & 1) * 128;
        if (t < 5) asm volatile("global_load_dwordx4 %0, %1, %2" : "+v"(pkv) : "v"(pko), "s"(src) : "memory"); };
#define ITER(t, E0, E1, WAITN) do { \
        const int s1 = (s0 == 2 * SLOT) ? 0 : s0 + SLOT;         \
        const int s2 = (s0 == 0) ? 2 * SLOT : s0 - SLOT;         \
        const char* ks_ = kbase + (size_t)((64 * ((t) + 2) + 32 + 8 * wid) & (SEQ - 1)) * (DH * 2); \
        const char* vs_ = vbase + (size_t)((64 * ((t) + 2) + 8 * wid) & (SEQ - 1)) * (DH * 2); \
        HSTEP(SA, SB, s1, 0, s0, 0, s1, 1, gldss(kvo, ks_, kdst + s0), gldss(kvo, ks_ + 128, kdst + s0 + 1024), gldss(kvo, ks_ + 256, kdst + s0 + 2048), \
              gldss(vvo, vs_, vdst + s2), gldss(vvo, vs_ + 128, vdst + s2 + 1024), gldss(vvo, vs_ + 256, vdst + s2 + 2048)); \
        HSTEP(SB, SA, s1, 1, s0, 1, s2, 0, (void)0, (void)0, E0, E1, (void)0, (void)0); \
        WAIT_BAR(WAITN); \
        s0 = s1; } while (0)
    for (int t = 0; t < 6; ++t) ITER(t, pk_store(t), pk_load(t), 3);
    for (int t = 6; t < 32; ++t) ITER(t, (void)0, (void)0, 3);
    asm volatile("s_waitcnt vmcnt(0)" ::: "memory");
    {
        const float il = __builtin_amdgcn_rcpf(xhalf_sum(lsum));
        char* obase = (char*)o + ((size_t)(row0 >> 5) * 384 + head * 12) * 1024 + r32 * 16 + 8 * hi;
#pragma unroll
        for (int db = 0; db < 6; ++db)
#pragma unroll
            for (int g = 0; g < 4; ++g) {
                u32x2 w; w[0] = cvtpk(o6[db][4 * g] * il, o6[db][4 * g + 1] * il); w[1] = cvtpk(o6[db][4 * g + 2] * il, o6[db][4 * g + 3] * il);
                *(u32x2*)(obase + (2 * db + (g >> 1)) * 1024 + (g & 1) * 512) = w;
            }
    }
}

constexpr int K2_STAGE = 16384, K2_LDS = 8 * K2_STAGE;
DI void st16_sc1(const void* sb, unsigned voff, f32x4 v) { asm volatile("global_store_dwordx4 %0, %1, %2 sc1\n\ts_nop 1" :: "v"(voff), "v"(v), "s"(sb) : "memory"); }
template <int OFF> DI void ld16_sc1(f32x4& d, unsigned voff, const void* sb) { asm volatile("global_load_dwordx4 %0, %1, %2 offset:%3 sc1" : "=v"(d) : "v"(voff), "s"(sb), "i"(OFF) : "memory"); }
__global__ __launch_bounds__(512, 2)
void k2_gemm(const u16* __restrict__ o, const u16* __restrict__ hbuf, const u16* __restrict__ wpk, float* __restrict__ part, unsigned* __restrict__ cnt,
             const u16* __restrict__ x, const u16* __restrict__ b2, u16* __restrict__ out
             ) {
    extern __shared__ __attribute__((aligned(16))) char lds[];
    const int tid = threadIdx.x, lane = tid & 63, r32 = lane & 31, hi = lane >> 5;
    const int wid = __builtin_amdgcn_readfirstlane(tid >> 6), wm = wid & 3, kh = wid >> 2;
    const int bid = blockIdx.x;
    const int lin = (bid & 7) * 30 + (bid >> 3);
    const int chunk = lin / 80, tile = lin - chunk * 80, tm = tile / 5, tn = tile - tm * 5;
    const unsigned lds0 = (unsigned)(uintptr_t)lds;
    const lds_cptr L = (lds_cptr)lds;
    const char* Ao = (const char*)o + ((size_t)(4 * tm + wm) * 384 + 2 * kh) * 1024;
    const char* Ah = (const char*)hbuf + ((size_t)(4 * tm + wm) * 96 + 2 * kh) * 1024;
    const char* Wp = (const char*)wpk + (size_t)tn * 120 * 16384 + wid * 2048;
    const unsigned avo = lane * 16;
    const unsigned wdst = lds0 + wid * 2048;
    const int g0 = chunk * 40;
    const int q4 = (lane & 15) >> 2, p4 = lane & 3, g1 = (lane >> 4) & 1;
    const lds_cptr wp = L + (4 * kh + hi) * 2048 + q4 * 64 + g1 * 32 + p4 * 8;
    f32x16 acc[4], sv[4];
#pragma unroll
    for (int i = 0; i < 4; ++i) acc[i] = f32x16{};
    bf16x8 ab[8][2];
#define SRC2(st) const int g_ = g0 + (((st) < 40) ? (st) : 39); const char* w_ = Wp + (size_t)g_ * 16384; \
        const char* a_ = (g_ < 96) ? Ao + (size_t)g_ * 4096 : Ah + (size_t)(g_ - 96) * 4096;
#define ISSUE2(st, sl) do { SRC2(st); gld16<0>(ab[sl][0], avo, a_); gld16<1024>(ab[sl][1], avo, a_); \
        gldss(avo, w_, wdst + (sl) * K2_STAGE); gldss(avo, w_ + 1024, wdst + (sl) * K2_STAGE + 1024); } while (0)
    ISSUE2(0, 0); ISSUE2(1, 1); ISSUE2(2, 2); ISSUE2(3, 3); ISSUE2(4, 4); ISSUE2(5, 5);
#define WF(sl, s, ni) cat(vtr(wq_[(sl) >> 1] + ((sl) & 1) * K2_STAGE + (s) * 4096 + ((ni) >> 1) * 1024 + ((ni) & 1) * 512), vtr(wq_[(sl) >> 1] + ((sl) & 1) * K2_STAGE + (s) * 4096 + ((ni) >> 1) * 1024 + ((ni) & 1) * 512 + 256))
#define NS(sl) (((sl) + 6) & 7)
#define N1(sl) (((sl) + 1) & 7)
    lds_cptr wq_[4];
#pragma unroll
    for (int i = 0; i < 4; ++i) { wq_[i] = wp + 2 * i * K2_STAGE; PIN(wq_[i]); }
#define STAGE2(st, sl) do { \
        asm volatile("s_waitcnt vmcnt(16) lgkmcnt(0)\n\ts_barrier" : "+v"(ab[sl][0]), "+v"(ab[sl][1]) :: "memory"); \
        SRC2((st) + 6); \
        f3 = WF(sl, 0, 3); acc[0] = MFMA(f0, ab[sl][0], acc[0]); PIN(acc[0]); gld16<0>(ab[NS(sl)][0], avo, a_); GEND2(); \
        f0 = WF(sl, 1, 0); acc[1] = MFMA(f1, ab[sl][0], acc[1]); PIN(acc[1]); gld16<1024>(ab[NS(sl)][1], avo, a_); GEND2(); \
        f1 = WF(sl, 1, 1); acc[2] = MFMA(f2, ab[sl][0], acc[2]); PIN(acc[2]); gldss(avo, w_, wdst + NS(sl) * K2_STAGE); GEND2(); \
        f2 = WF(sl, 1, 2); acc[3] = MFMA(f3, ab[sl][0], acc[3]); PIN(acc[3]); GEND2(); \
        f3 = WF(sl, 1, 3); acc[0] = MFMA(f0, ab[sl][1], acc[0]); PIN(acc[0]); gldss(avo, w_ + 1024, wdst + NS(sl) * K2_STAGE + 1024); GEND2(); \
        f0 = WF(N1(sl), 0, 0); acc[1] = MFMA(f1, ab[sl][1], acc[1]); PIN(acc[1]); GEND2(); \
        f1 = WF(N1(sl), 0, 1); acc[2] = MFMA(f2, ab[sl][1], acc[2]); PIN(acc[2]); GEND2(); \
        f2 = WF(N1(sl), 0, 2); acc[3] = MFMA(f3, ab[sl][1], acc[3]); PIN(acc[3]); GEND2(); \
    } while (0)
#define GEND2() do { asm volatile("" ::: "memory"); SBAR(); } while (0)
    asm volatile("s_waitcnt vmcnt(20)\n\ts_barrier" ::: "memory");
    bf16x8 f0 = WF(0, 0, 0), f1 = WF(0, 0, 1), f2 = WF(0, 0, 2), f3;
#define STAGE8(s8) do { STAGE2(s8, 0); STAGE2(s8 + 1, 1); STAGE2(s8 + 2, 2); STAGE2(s8 + 3, 3); STAGE2(s8 + 4, 4); STAGE2(s8 + 5, 5); STAGE2(s8 + 6, 6); STAGE2(s8 + 7, 7); } while (0)
    for (int s8 = 0; s8 < 16; s8 += 8) STAGE8(s8);
#pragma unroll
    for (int i = 0; i < 4; ++i) { sv[i] = acc[i]; if (chunk == 2) acc[i] = f32x16{}; }
    for (int s8 = 16; s8 < 40; s8 += 8) STAGE8(s8);
    asm volatile("s_waitcnt vmcnt(0) lgkmcnt(0)\n\ts_barrier" : "+v"(ab[0][0]), "+v"(ab[0][1]), "+v"(ab[1][0]), "+v"(ab[1][1]), "+v"(ab[2][0]), "+v"(ab[2][1]),
                 "+v"(ab[3][0]), "+v"(ab[3][1]), "+v"(ab[4][0]), "+v"(ab[4][1]), "+v"(ab[5][0]), "+v"(ab[5][1]), "+v"(f0), "+v"(f1), "+v"(f2) :: "memory");
    char* pbase = (char*)part + (size_t)tile * 4 * 65536 + wid * 8192;
    float* xw = (float*)(lds + wid * 8192); const float* xr = (const float*)(lds + (wid ^ 4) * 8192);
    auto exchange = [&](const f32x16 (&ac)[4], f32x4 (&res)[8]) {
#pragma unroll
        for (int ni = 0; ni < 2; ++ni)
#pragma unroll
            for (int g = 0; g < 4; ++g) { const f32x16& a_ = kh ? ac[ni] : ac[2 + ni]; f32x4 v = {a_[4 * g], a_[4 * g + 1], a_[4 * g + 2], a_[4 * g + 3]}; *(f32x4*)(xw + ((ni * 4 + g) * 64 + lane) * 4) = v; }
        asm volatile("s_waitcnt vmcnt(0) lgkmcnt(0)\n\ts_barrier" ::: "memory");
#pragma unroll
        for (int ni = 0; ni < 2; ++ni)
#pragma unroll
            for (int g = 0; g < 4; ++g) { const f32x16& a_ = kh ? ac[2 + ni] : ac[ni];
                const f32x4 o_ = *(const f32x4*)(xr + ((ni * 4 + g) * 64 + lane) * 4);
                f32x4 v;
                if (kh) { v[0] = o_[0] + a_[4 * g]; v[1] = o_[1] + a_[4 * g + 1]; v[2] = o_[2] + a_[4 * g + 2]; v[3] = o_[3] + a_[4 * g + 3]; }
                else    { v[0] = a_[4 * g] + o_[0]; v[1] = a_[4 * g + 1] + o_[1]; v[2] = a_[4 * g + 2] + o_[2]; v[3] = a_[4 * g + 3] + o_[3]; }
                res[ni * 4 + g] = v; }
        asm volatile("s_waitcnt lgkmcnt(0)\n\ts_barrier" ::: "memory");
    };
    const int row = tm * 128 + wm * 32 + r32, col0 = tn * 128 + kh * 64 + 4 * hi;
    u32x2 xb[4], bb[4];
#pragma unroll
    for (int u = 0; u < 4; ++u) { const int i = 2 * chunk + u; xb[u] = *(const u32x2*)(x + (size_t)row * DM + col0 + 32 * (i >> 2) + 8 * (i & 3)); bb[u] = *(const u32x2*)(b2 + col0 + 32 * (i >> 2) + 8 * (i & 3)); }
    f32x4 ra[8], rb[8];
    if (chunk == 2) { exchange(sv, ra); exchange(acc, rb); } else exchange(acc, ra);
    auto finish = [&](auto CH) {
        constexpr int C = decltype(CH)::value;
        constexpr int NU = (C == 2) ? 4 : 2, U0 = 2 * C;
#pragma unroll
        for (int i = 0; i < 8; ++i) if (i < U0 || i >= U0 + NU) st16_sc1(pbase + (size_t)C * 65536 + i * 1024, avo, ra[i]);
        if constexpr (C == 2) {
#pragma unroll
            for (int i = 0; i < 4; ++i) st16_sc1(pbase + (size_t)3 * 65536 + i * 1024, avo, rb[i]);
        }
        asm volatile("s_waitcnt vmcnt(0)\n\ts_barrier" ::: "memory");
        if (tid == 0) {
            __hip_atomic_fetch_add(cnt + tile * 64, 1u, __ATOMIC_RELAXED, __HIP_MEMORY_SCOPE_AGENT);
            for (int spin = 0; spin < (1 << 22); ++spin) {
                if (__hip_atomic_load(cnt + tile * 64, __ATOMIC_RELAXED, __HIP_MEMORY_SCOPE_AGENT) >= 3u) break;
                __builtin_amdgcn_s_sleep(1);
            }
        }
        asm volatile("s_waitcnt vmcnt(0) lgkmcnt(0)\n\ts_barrier" ::: "memory");
        constexpr int SA_ = (C == 0) ? 1 : 0, SB_ = (C == 2) ? 1 : 2;
        f32x4 pa[4], pb[4], pg[2];
#pragma unroll
        for (int u = 0; u < NU; ++u) { const int i = U0 + u;
            ld16_sc1<0>(pa[u], avo, pbase + (size_t)SA_ * 65536 + i * 1024); ld16_sc1<0>(pb[u], avo, pbase + (size_t)SB_ * 65536 + i * 1024);
            if constexpr (C != 2) ld16_sc1<0>(pg[u], avo, pbase + (size_t)3 * 65536 + i * 1024); }
        if constexpr (C != 2) asm volatile("s_waitcnt vmcnt(0)" : "+v"(pa[0]), "+v"(pa[1]), "+v"(pb[0]), "+v"(pb[1]), "+v"(pg[0]), "+v"(pg[1]) :: "memory");
        else                  asm volatile("s_waitcnt vmcnt(0)" : "+v"(pa[0]), "+v"(pa[1]), "+v"(pa[2]), "+v"(pa[3]), "+v"(pb[0]), "+v"(pb[1]), "+v"(pb[2]), "+v"(pb[3]) :: "memory");
#pragma unroll
        for (int u = 0; u < NU; ++u) { const int i = U0 + u;
            float r[4];
#pragma unroll
            for (int j = 0; j < 4; ++j) {
                const float xv = (j & 1) ? __uint_as_float(xb[u][j >> 1] & 0xffff0000u) : __uint_as_float(xb[u][j >> 1] << 16);
                const float bv = (j & 1) ? __uint_as_float(bb[u][j >> 1] & 0xffff0000u) : __uint_as_float(bb[u][j >> 1] << 16);
                const float p0 = (C == 0) ? ra[i][j] : pa[u][j];
                const float p1 = (C == 0) ? pa[u][j] : (C == 1) ? ra[i][j] : pb[u][j];
                const float p2 = (C == 2) ? ra[i][j] : pb[u][j];
                const float gv = (C == 2) ? rb[i][j] : pg[u][j];
                const float s = rbf((p0 + p1) + p2);
                const float mm = rbf(rbf(gv) + bv);
                const float gt = rbf(mm * __builtin_amdgcn_rcpf(1.f + __builtin_amdgcn_exp2f(-mm * 1.4426950408889634f)));
                r[j] = xv + rbf(s * gt);
            }
            u32x2 w; w[0] = cvtpk(r[0], r[1]); w[1] = cvtpk(r[2], r[3]);
            *(u32x2*)(out + (size_t)row * DM + col0 + 32 * (i >> 2) + 8 * (i & 3)) = w;
        }
    };
    if (chunk == 0) finish(std::integral_constant<int, 0>{}); else if (chunk == 1) finish(std::integral_constant<int, 1>{}); else finish(std::integral_constant<int, 2>{});
}

extern "C" void kernel_launch(void* const* d_in, const int* in_sizes, int n_in, void* d_out, int out_size, void* d_ws, size_t ws_size, hipStream_t stream) {
    const u16* x  = (const u16*)d_in[0];
    const u16* kc = (const u16*)d_in[1];
    const u16* vc = (const u16*)d_in[2];
    const u16* wq = (const u16*)d_in[3];
    const u16* wo = (const u16*)d_in[4];
    const u16* w1 = (const u16*)d_in[6];
    const u16* b1 = (const u16*)d_in[7];
    const u16* w2 = (const u16*)d_in[8];
    const u16* b2 = (const u16*)d_in[9];
    char* ws = (char*)d_ws;
    u16* xn = (u16*)ws;
    float* st2 = (float*)(ws + (size_t)4 * 1024 * 1024);
    float* w1s = (float*)(ws + (size_t)5 * 1024 * 1024);
    u16* hb = (u16*)(ws + (size_t)8 * 1024 * 1024);
    u16* ob = (u16*)(ws + (size_t)16 * 1024 * 1024);
    u16* wpk = (u16*)(ws + (size_t)96 * 1024 * 1024);
    unsigned* cnt = (unsigned*)(ws + (size_t)6 * 1024 * 1024);
    float* part = (float*)(ws + (size_t)48 * 1024 * 1024);
    (void)hipFuncSetAttribute((const void*)k1_attn, hipFuncAttributeMaxDynamicSharedMemorySize, K1_LDS);
    (void)hipFuncSetAttribute((const void*)k2_gemm, hipFuncAttributeMaxDynamicSharedMemorySize, K2_LDS);
    hipLaunchKernelGGL(k0_ln, dim3(K0_LN + K0_WS), dim3(256), 0, stream, x, w1, xn, st2, w1s, cnt);
    hipLaunchKernelGGL(k1_attn, dim3(256), dim3(512), K1_LDS, stream, xn, st2, w1s, kc, vc, wq, w1, b1, ob, hb, wo, w2, wpk
        );
    hipLaunchKernelGGL(k2_gemm, dim3(240), dim3(512), K2_LDS, stream, ob, hb, wpk, part, cnt, x, b2, (u16*)d_out
        );
}
```
